```python
import torch

def _softmax(x, axis=-1):
    return torch.softmax(x, dim=axis)
def _gelu(x):
    return torch.nn.functional.gelu(x, approximate="tanh")
def _silu(x):
    return torch.nn.functional.silu(x)
def _sigmoid(x):
    return torch.sigmoid(x)
def _layernorm(x, axis=-1, eps=1e-5):
    m = x.mean(dim=axis, keepdim=True)
    v = x.var(dim=axis, keepdim=True, unbiased=False)
    return (x - m) / torch.sqrt(v + eps)
def _scatter_add(tgt, idx, src, axis=0):
    return tgt.clone().index_add_(axis, idx, src)
def _segment_sum(src, seg, n):
    out = torch.zeros((n,) + src.shape[1:], dtype=src.dtype)
    return out.index_add_(0, seg, src)
def _pad(x, pw):
    flat = tuple(v for pair in reversed(pw) for v in pair)
    return torch.nn.functional.pad(x, flat)

kernel_name = 'sequential_preLN__mha_block__geglu_ffn__prompt'

def setup_inputs():

    _w = (lambda a: (lambda t: t.to(torch.bfloat16) if t.is_floating_point() else t)(torch.as_tensor(a)))
    def _A_setup():
        B, S, D, H = 1, 2048, 640, 32
        return {
            "x": _w((lambda *s: torch.randn(*s, dtype=torch.bfloat16) * 0.05)(B, S, D)),
            "wqkv": _w((lambda *s: torch.randn(*s, dtype=torch.bfloat16) * 0.05)(D, 3 * D)),
            "wo": _w((lambda *s: torch.randn(*s, dtype=torch.bfloat16) * 0.05)(D, D)),
        }
    _A_in = _A_setup()

    def _B_setup():
        B, S, D, F = 1, 2048, 640, 1536
        return {
            "x": _w((lambda *s: torch.randn(*s, dtype=torch.bfloat16) * 0.05)(B, S, D)),
            "w_gate": _w((lambda *s: torch.randn(*s, dtype=torch.bfloat16) * 0.05)(D, F)),
            "w_up": _w((lambda *s: torch.randn(*s, dtype=torch.bfloat16) * 0.05)(D, F)),
            "w_down": _w((lambda *s: torch.randn(*s, dtype=torch.bfloat16) * 0.05)(F, D)),
        }
    _B_in = _B_setup()

    return {
        "A_x": _A_in["x"],
        "A_wqkv": _A_in["wqkv"],
        "A_wo": _A_in["wo"],
        "B_x": _B_in["x"],
        "B_w_gate": _B_in["w_gate"],
        "B_w_up": _B_in["w_up"],
        "B_w_down": _B_in["w_down"],
    }

def reference(A_x, A_wqkv, A_wo, B_x, B_w_gate, B_w_up, B_w_down):
    def _A_ref(x, wqkv, wo):
        B, S, D = x.shape
        H = 32
        Dh = D // H
        qkv = (lambda x, *d: x.permute(*d))((x @ wqkv).reshape(B, S, 3, H, Dh), 2, 0, 3, 1, 4)
        q, k, v = qkv[0], qkv[1], qkv[2]
        scores = torch.einsum("bhqd,bhkd->bhqk", q, k) * (Dh ** -0.5)
        p = _softmax(scores, axis=-1)
        o = torch.einsum("bhqk,bhkd->bhqd", p, v)
        return (lambda x, *d: x.permute(*d))(o, 0, 2, 1, 3).reshape(B, S, D) @ wo

    def _B_ref(x, w_gate, w_up, w_down):
        return (_gelu(x @ w_gate) * (x @ w_up)) @ w_down

    x = A_x
    h = x + _A_ref(_layernorm(x, axis=-1), A_wqkv, A_wo)
    h2 = h + _B_ref(_layernorm(h, axis=-1), B_w_gate, B_w_up, B_w_down)
    return h2
```

```cpp
#include <hip/hip_runtime.h>
#include <hip/hip_bf16.h>
#include <stdint.h>

namespace blk {

typedef short bf16x8 __attribute__((ext_vector_type(8)));
typedef short bf16x4 __attribute__((ext_vector_type(4)));
typedef float f32x2 __attribute__((ext_vector_type(2)));
typedef float f32x4 __attribute__((ext_vector_type(4)));
typedef float f32x16 __attribute__((ext_vector_type(16)));
typedef float f32x32 __attribute__((ext_vector_type(32)));
typedef unsigned u32x2 __attribute__((ext_vector_type(2)));
typedef unsigned u32x4 __attribute__((ext_vector_type(4)));
typedef unsigned short u16;
#define LAS __attribute__((address_space(3)))
#define DI __device__ __forceinline__

constexpr int SEQ = 2048, DM = 640, NH = 32, DH = 20, FF = 1536, NQKV = 1920;
constexpr float LN_EPS = 1e-5f;
constexpr float QSCALE = 0.22360679774997896f * 1.4426950408889634f;

constexpr int Q_TILE = 1280;
constexpr int KV_STEP = 5184;
constexpr int KV_HEAD = 32 * KV_STEP;
constexpr size_t QI_OFF = 0;
constexpr size_t KVI_OFF = QI_OFF + (size_t)NH * 64 * Q_TILE;
constexpr size_t OI_OFF = KVI_OFF + (size_t)NH * KV_HEAD + 4096;
constexpr size_t HI_OFF = OI_OFF + (size_t)128 * 20 * 1024;
constexpr size_t GI_OFF = HI_OFF + (size_t)64 * 40 * 1024;
constexpr size_t WOT_OFF = GI_OFF + (size_t)128 * 48 * 1024;
constexpr size_t WGU_OFF = WOT_OFF + (size_t)40 * 20 * 1024;
constexpr size_t WDT_OFF = WGU_OFF + (size_t)96 * 40 * 1024;
constexpr size_t WS_END = WDT_OFF + (size_t)40 * 48 * 1024;

template <int N> struct IC { static constexpr int v = N; };
template <int B, int E, class F> DI void static_for(F&& f) { if constexpr (B < E) { f(IC<B>{}); static_for<B + 1, E>(f); } }
template <int N> DI void wait_vm() { asm volatile("s_waitcnt vmcnt(%0)" :: "n"(N) : "memory"); }
DI void wait_lgkm0() { asm volatile("s_waitcnt lgkmcnt(0)" ::: "memory"); }
DI void bar() { asm volatile("s_barrier" ::: "memory"); }
DI void bar_lgkm() { asm volatile("s_waitcnt lgkmcnt(0)\n\ts_barrier" ::: "memory"); }
DI void glds16(const void* sbase, unsigned voff, unsigned lds_dst) {
    unsigned keep;
    asm volatile("s_mov_b32 %0, m0\n\ts_mov_b32 m0, %3\n\ts_nop 0\n\tglobal_load_lds_dwordx4 %1, %2\n\ts_mov_b32 m0, %0"
                 : "=&s"(keep) : "v"(voff), "s"(sbase), "s"(lds_dst) : "memory");
}
typedef __bf16 bfv2 __attribute__((ext_vector_type(2)));
DI unsigned cvtpk(float lo, float hi) { const f32x2 v = {lo, hi}; return __builtin_bit_cast(unsigned, __builtin_convertvector(v, bfv2)); }
DI float dot2bf(unsigned a, unsigned b, float c) { return __builtin_amdgcn_fdot2_f32_bf16(__builtin_bit_cast(bfv2, a), __builtin_bit_cast(bfv2, b), c, false); }
DI float bf_lo(unsigned w) { return __uint_as_float(w << 16); }
DI float bf_hi(unsigned w) { return __uint_as_float(w & 0xffff0000u); }
DI bf16x8 lds_frag(unsigned addr) { return *(const LAS bf16x8*)(uintptr_t)addr; }
DI bf16x4 lds_frag4(unsigned addr) { return *(const LAS bf16x4*)(uintptr_t)addr; }
DI bf16x4 lds_tr(unsigned addr) {
    typedef short v4i16_t __attribute__((ext_vector_type(4)));
    return __builtin_amdgcn_ds_read_tr16_b64_v4i16((LAS v4i16_t*)(uintptr_t)addr);
}
#define MFMA32(a, b, c) __builtin_amdgcn_mfma_f32_32x32x16_bf16((a), (b), (c), 0, 0, 0)
#define MFMA16(a, b, c) __builtin_amdgcn_mfma_f32_16x16x32_bf16((a), (b), (c), 0, 0, 0)
#define MFMA2B(a, b, c) __builtin_amdgcn_mfma_f32_32x32x4bf16_1k((a), (b), (c), 0, 0, 0)
DI float xhalf_add(float v) { return v + __shfl_xor(v, 32); }
DI void stat_sum(const bf16x8& f, float& s) { const u32x4 w = __builtin_bit_cast(u32x4, f);
#pragma unroll
    for (int i = 0; i < 4; ++i) s = dot2bf(w[i], 0x3F803F80u, s); }
DI void stat_sq(const bf16x8& f, float& q) { const u32x4 w = __builtin_bit_cast(u32x4, f);
#pragma unroll
    for (int i = 0; i < 4; ++i) q = dot2bf(w[i], w[i], q); }
DI bf16x8 split_a(float a, int h) { const unsigned hi = cvtpk(a, a); const float lo = a - bf_lo(hi); u32x4 w = {hi, cvtpk(lo, 0.f), 0u, 0u}; if (h) w = (u32x4){0u, 0u, 0u, 0u}; return __builtin_bit_cast(bf16x8, w); }
DI bf16x8 split_b(float b, int h) { const unsigned hi = cvtpk(b, 0.f); const float lo = b - bf_lo(hi); u32x4 w = {cvtpk(b, lo), hi, 0u, 0u}; if (h) w = (u32x4){0u, 0u, 0u, 0u}; return __builtin_bit_cast(bf16x8, w); }

template <int NST, int NS, int L, class Issue> DI void loader_loop(Issue&& issue) {
    constexpr int PRE = NS < NST ? NS : NST;
    static_for<0, PRE>([&](auto s) { issue(s.v, s.v); });
    wait_vm<(PRE - 1) * L>(); bar();
    static_for<0, NST - 1>([&](auto s) {
        constexpr int up = (s.v - 1 + NS) < (NST - 1) ? (s.v - 1 + NS) : (NST - 1);
        wait_vm<(up - (s.v + 1)) * L>(); bar();
        if constexpr (s.v + NS < NST) issue(s.v + NS, s.v % NS);
    });
}

constexpr int RT_WO = 80 * 5, RT_WD = 192 * 5, RT_WG = 80 * 12, RT_JOBS = RT_WO + RT_WD + 2 * RT_WG;
DI void retile_job(int job, int lane, const u16* wo, const u16* wg, const u16* wu, const u16* wd, char* ws) {
    const u16* src; int ld, ra, rb, nb; char* dst; int tile_stride, tw16;
    if (job < RT_WO) { const int ci = job / 5; nb = job % 5; const int p = ci / 5, r = ci % 5; src = wo; ld = DM;
        if (r < 4) { ra = 20 * (2 * p + (r >> 1)) + 4 * (r & 1); rb = ra + 8; } else { ra = 40 * p + 16; rb = 40 * p + 36; }
        dst = ws + WOT_OFF + (ci >> 2) * 1024 + (ci & 3) * 256; tile_stride = 20 * 1024; tw16 = 1;
    } else if (job < RT_WO + RT_WD) { const int j = job - RT_WO, ci = j / 5; nb = j % 5; src = wd; ld = DM;
        const int kb = ci >> 2, g = ci & 3; ra = 16 * (2 * kb + (g >> 1)) + 4 * (g & 1); rb = ra + 8;
        dst = ws + WDT_OFF + kb * 1024 + g * 256; tile_stride = 48 * 1024; tw16 = 1;
    } else { int j = job - RT_WO - RT_WD; const int u = j >= RT_WG; j -= u * RT_WG; const int ci = j / 12; nb = j % 12; src = u ? wu : wg; ld = FF;
        ra = 8 * ci; rb = ra + 4; dst = ws + WGU_OFF + (ci >> 1) * 1024 + (ci & 1) * 512 + u * 256; tile_stride = 40 * 1024; tw16 = 0; }
    (void)tw16;
    const int n = nb * 128 + 2 * lane;
    unsigned v[8];
#pragma unroll
    for (int j = 0; j < 8; ++j) v[j] = *(const unsigned*)(src + (size_t)((j < 4 ? ra : rb - 4) + j) * ld + n);
    u32x4 e, o;
#pragma unroll
    for (int j = 0; j < 4; ++j) { e[j] = __builtin_amdgcn_perm(v[2 * j + 1], v[2 * j], 0x05040100u); o[j] = __builtin_amdgcn_perm(v[2 * j + 1], v[2 * j], 0x07060302u); }
    char* d = dst + (size_t)(n >> 4) * tile_stride + (n & 15) * 16;
    *(u32x4*)d = e; *(u32x4*)(d + 16) = o;
}
__global__ __launch_bounds__(256) void k_retile(const u16* wo, const u16* wg, const u16* wu, const u16* wd, char* ws) {
    const int job = blockIdx.x * 4 + (threadIdx.x >> 6);
    if (job < RT_JOBS) retile_job(job, threadIdx.x & 63, wo, wg, wu, wd, ws);
}

constexpr int K1_NS = 4, K1_SLOT = 32768, K1_NST = 10;
template <bool VT> DI void k1_compute(unsigned lds0, int wid, int lane, int m0, int n0, char* ws) {
    const int wr = wid >> 1, wc = wid & 1, r32 = lane & 31, h = lane >> 5;
    unsigned xa[4];
    { const int row = wr * 64 + wc * 32 + r32;
#pragma unroll
      for (int ks = 0; ks < 4; ++ks) xa[ks] = lds0 + row * 128 + (((2 * ks + h) ^ ((row >> 1) & 7)) << 4); }
    const int xo1 = wc ? -4096 : 4096;
    unsigned wa[2];
    { const int G = lane >> 4, q = (lane & 15) >> 2, p = lane & 3;
#pragma unroll
      for (int t = 0; t < 2; ++t) { const int nt = wr ^ t; const int c = wc * 8 + nt * 4 + (G & 1) * 2 + (p >> 1);
          wa[t] = lds0 + 16384 + (8 * (G >> 1) + q) * 256 + ((c ^ (q << 2)) << 4) + (p & 1) * 8; } }
    f32x16 acc[2][2] = {};
    float s1 = 0.f, s2 = 0.f, cs = 0.f;
    bf16x8 xf[2][2], wf[2][2];
    auto load = [&](int buf, unsigned sl, int ks) {
        xf[buf][0] = lds_frag(xa[ks] + sl); xf[buf][1] = lds_frag(xa[ks] + sl + xo1);
#pragma unroll
        for (int t = 0; t < 2; ++t) { const bf16x4 a = lds_tr(wa[t] + sl + ks * 4096), b = lds_tr(wa[t] + sl + ks * 4096 + 1024);
            wf[buf][t] = __builtin_shufflevector(a, b, 0, 1, 2, 3, 4, 5, 6, 7); }
    };
    auto mma = [&](int buf) {
#pragma unroll
        for (int t = 0; t < 2; ++t)
#pragma unroll
            for (int i = 0; i < 2; ++i) acc[t][i] = VT ? MFMA32(xf[buf][i], wf[buf][t], acc[t][i]) : MFMA32(wf[buf][t], xf[buf][i], acc[t][i]);
        stat_sum(xf[buf][0], s1); stat_sq(xf[buf][0], s2); stat_sum(wf[buf][0], cs);
    };
    bar();
    load(0, 0, 0);
    unsigned sl = 0;
    for (int s = 0; s < K1_NST; ++s) {
        load(1, sl, 1); mma(0);
        load(0, sl, 2); mma(1);
        load(1, sl, 3); mma(0);
        sl = (sl + K1_SLOT) & (K1_NS * K1_SLOT - 1);
        if (s < K1_NST - 1) { bar_lgkm(); load(0, sl, 0); }
        mma(1);
    }
    LAS float* st = (LAS float*)(uintptr_t)(lds0 + 2 * K1_SLOT);
    s1 = xhalf_add(s1); s2 = xhalf_add(s2); cs = xhalf_add(cs);
    { const float mean = s1 * (1.f / DM); const float var = fmaxf(s2 * (1.f / DM) - mean * mean, 0.f);
      if (h == 0) { st[wr * 64 + wc * 32 + r32] = mean; st[128 + wr * 64 + wc * 32 + r32] = __builtin_amdgcn_rsqf(var + LN_EPS); st[256 + wc * 64 + wr * 32 + r32] = cs; } }
    bar_lgkm();
    bf16x8 mu[2], cf[2];
#pragma unroll
    for (int i = 0; i < 2; ++i) { mu[i] = split_b(-st[wr * 64 + (wc ^ i) * 32 + r32], h); cf[i] = split_a(st[256 + wc * 64 + (wr ^ i) * 32 + r32], h); }
#pragma unroll
    for (int t = 0; t < 2; ++t)
#pragma unroll
        for (int i = 0; i < 2; ++i) acc[t][i] = VT ? MFMA32(mu[i], cf[t], acc[t][i]) : MFMA32(cf[t], mu[i], acc[t][i]);
    if constexpr (!VT) {
        const bool isq = n0 < DM; const int sec0 = n0 - (isq ? 0 : DM);
#pragma unroll
        for (int i = 0; i < 2; ++i) {
            const int tl = wr * 64 + (wc ^ i) * 32 + r32, tok = m0 + tl;
            const float rs = st[128 + tl] * (isq ? QSCALE : 1.f);
            char* base = isq ? ws + QI_OFF + (size_t)(tok >> 5) * Q_TILE : ws + KVI_OFF + (size_t)(tok >> 6) * KV_STEP;
            const int mainoff = (isq ? 0 : ((tok >> 5) & 1) * 1024) + r32 * 16, tailoff = isq ? 1024 + r32 * 8 : 2048 + (tok & 63) * 8;
            const int hstride = isq ? 64 * Q_TILE : KV_HEAD;
#pragma unroll
            for (int t = 0; t < 2; ++t)
#pragma unroll
                for (int g = 0; g < 4; ++g) {
                    const int col = sec0 + wc * 64 + (wr ^ t) * 32 + 8 * g + 4 * h, head = col / 20, d0 = col - head * 20;
                    const u32x2 w = {cvtpk(acc[t][i][4 * g] * rs, acc[t][i][4 * g + 1] * rs), cvtpk(acc[t][i][4 * g + 2] * rs, acc[t][i][4 * g + 3] * rs)};
                    *(u32x2*)(base + (size_t)head * hstride + (d0 < 16 ? mainoff + (d0 >> 3) * 512 + (d0 & 7) * 2 : tailoff)) = w;
                }
        }
    } else {
#pragma unroll
        for (int i = 0; i < 2; ++i) {
            const int tl = wr * 64 + (wc ^ i) * 32, tok0 = m0 + tl;
            f32x4 rs[4];
#pragma unroll
            for (int g = 0; g < 4; ++g) rs[g] = *(const LAS f32x4*)(st + 128 + tl + 8 * g + 4 * h);
#pragma unroll
            for (int t = 0; t < 2; ++t) {
                const int col = n0 - 2 * DM + wc * 64 + (wr ^ t) * 32 + r32, head = col / 20, d = col - head * 20;
                char* base = ws + KVI_OFF + (size_t)head * KV_HEAD + (size_t)(tok0 >> 6) * KV_STEP + 2560 + ((tok0 >> 5) & 1) * 1312;
#pragma unroll
                for (int s = 0; s < 2; ++s) {
                    u32x4 w;
#pragma unroll
                    for (int j = 0; j < 4; ++j) { const int r = 8 * s + 2 * j; w[j] = cvtpk(acc[t][i][r] * rs[r >> 2][r & 3], acc[t][i][r + 1] * rs[r >> 2][(r & 3) + 1]); }
                    *(u32x4*)(base + s * 656 + h * 320 + d * 16) = w;
                    if (d == 0 && h == 0) *(u32x4*)(base + s * 656 + 640) = (u32x4){0x3F803F80u, 0x3F803F80u, 0x3F803F80u, 0x3F803F80u};
                }
            }
        }
    }
}
__global__ __launch_bounds__(512, 2) void k_qkv(const u16* x, const u16* wqkv, char* ws) {
    __shared__ __attribute__((aligned(16))) char smem[K1_NS * K1_SLOT];
    const int xcd = blockIdx.x & 7, idx = blockIdx.x >> 3;
    const int rt = (xcd >> 1) * 4 + (idx & 3), ct = (xcd & 1) * 8 + (idx >> 2);
    if (ct >= 15) return;
    const int m0 = rt * 128, n0 = ct * 128;
    const int lane = threadIdx.x & 63, wid = __builtin_amdgcn_readfirstlane(threadIdx.x >> 6);
    const unsigned lds0 = (unsigned)(uintptr_t)smem;
    if (wid >= 4) {
        const int lw = wid - 4;
        unsigned xo[4], wo[4];
#pragma unroll
        for (int p = 0; p < 4; ++p) {
            const int row = (4 * lw + p) * 8 + (lane >> 3); xo[p] = (m0 + row) * (DM * 2) + (((lane & 7) ^ ((row >> 1) & 7)) << 4);
            const int kr = (4 * lw + p) * 4 + (lane >> 4); wo[p] = kr * (NQKV * 2) + n0 * 2 + (((lane & 15) ^ ((kr & 3) << 2)) << 4);
        }
        loader_loop<K1_NST, K1_NS, 8>([&](int st, int slot) {
            const char* xb = (const char*)x + st * 128; const char* wb = (const char*)wqkv + (size_t)st * 64 * NQKV * 2;
#pragma unroll
            for (int p = 0; p < 4; ++p) { glds16(wb, wo[p], lds0 + slot * K1_SLOT + 16384 + (4 * lw + p) * 1024); glds16(xb, xo[p], lds0 + slot * K1_SLOT + (4 * lw + p) * 1024); }
        });
        return;
    }
    if (ct >= 10) k1_compute<true>(lds0, wid, lane, m0, n0, ws); else k1_compute<false>(lds0, wid, lane, m0, n0, ws);
}

constexpr int AT_RES = 28;
constexpr int AT_LDS = 143 * 1024;
DI void attn_store(const f32x16& o, int lane, int head, int q0, char* ws) {
    const int r32 = lane & 31, h = lane >> 5;
    const float l = __shfl(o[8], r32 + 32);
    const float inv = 1.f / l;
    const int tok = q0 + r32, p = head >> 1, hb = head & 1;
    char* base = ws + OI_OFF + (size_t)(tok >> 4) * (20 * 1024) + (tok & 15) * 16;
    const u32x4 w = {cvtpk(o[0] * inv, o[1] * inv), cvtpk(o[2] * inv, o[3] * inv), cvtpk(o[4] * inv, o[5] * inv), cvtpk(o[6] * inv, o[7] * inv)};
    *(u32x4*)(base + (5 * p + 2 * hb + h) * 256) = w;
    if (h == 0) { const u32x2 t = {cvtpk(o[8] * inv, o[9] * inv), cvtpk(o[10] * inv, o[11] * inv)}; *(u32x2*)(base + (5 * p + 4) * 256 + hb * 8) = t; }
}
DI f32x32 qk_step(const bf16x8& k0, const bf16x8& k1, const bf16x4& kt, const bf16x8& qm, const bf16x4& qt, const f32x16& c) {
    const f32x16 a = MFMA32(k0, qm, c), b = MFMA32(k1, qm, c);
    f32x32 s = __builtin_shufflevector(a, b, 0, 1, 2, 3, 4, 5, 6, 7, 8, 9, 10, 11, 12, 13, 14, 15, 16, 17, 18, 19, 20, 21, 22, 23, 24, 25, 26, 27, 28, 29, 30, 31);
    return MFMA2B(kt, qt, s);
}
DI float max32(const f32x32& s) {
    float m = fmaxf(s[0], s[1]);
#pragma unroll
    for (int r = 2; r < 32; r += 2) m = fmaxf(fmaxf(m, s[r]), s[r + 1]);
    return fmaxf(m, __shfl_xor(m, 32));
}
DI bf16x8 pack8(const f32x32& p, int b) { const u32x4 w = {cvtpk(p[b], p[b + 1]), cvtpk(p[b + 2], p[b + 3]), cvtpk(p[b + 4], p[b + 5]), cvtpk(p[b + 6], p[b + 7])}; return __builtin_bit_cast(bf16x8, w); }
DI f32x16 attn_slow(const char* kv, int lane, int voff, bf16x8 qm, bf16x4 qt) {
    f32x16 o = {}; float m = -INFINITY; const f32x16 zero = {};
    for (int t = 0; t < 32; ++t) {
        const char* b = kv + t * KV_STEP;
        f32x32 s = qk_step(*(const bf16x8*)(b + lane * 16), *(const bf16x8*)(b + 1024 + lane * 16), *(const bf16x4*)(b + 2048 + lane * 8), qm, qt, zero);
        const float mn = fmaxf(m, max32(s)); const float f = __builtin_amdgcn_exp2f(m - mn); m = mn;
#pragma unroll
        for (int r = 0; r < 16; ++r) o[r] *= f;
#pragma unroll
        for (int r = 0; r < 32; ++r) s[r] = __builtin_amdgcn_exp2f(s[r] - m);
#pragma unroll
        for (int j = 0; j < 4; ++j) o = MFMA32(*(const bf16x8*)(b + voff + j * 656), pack8(s, 8 * j), o);
    }
    return o;
}
__global__ __launch_bounds__(512, 2) void k_attn(char* ws) {
    __shared__ __attribute__((aligned(16))) char smem[AT_LDS];
    const int xcd = blockIdx.x & 7, idx = blockIdx.x >> 3, head = xcd * 4 + (idx >> 3), qb = idx & 7;
    const int lane = threadIdx.x & 63, wid = __builtin_amdgcn_readfirstlane(threadIdx.x >> 6), r32 = lane & 31, h = lane >> 5;
    const unsigned lds0 = (unsigned)(uintptr_t)smem;
    const char* kv = ws + KVI_OFF + (size_t)head * KV_HEAD;
    const unsigned lo = lane * 16;
#pragma unroll
    for (int p = 0; p < 2; ++p) glds16(kv + (wid * 2 + p) * 1024, lo, lds0 + (wid * 2 + p) * 1024);
    const char* qp = ws + QI_OFF + (size_t)(head * 64 + qb * 8 + wid) * Q_TILE;
    bf16x8 qm; bf16x4 qt;
    asm volatile("global_load_dwordx4 %0, %2, %3\n\tglobal_load_dwordx2 %1, %4, %3 offset:1024" : "=&v"(qm), "=&v"(qt) : "v"(lo), "s"(qp), "v"(r32 * 8) : "memory");
#pragma unroll
    for (int p = 0; p < 16; ++p) { const int pc = 16 + p * 8 + wid; if (pc < 142) glds16(kv + pc * 1024, lo, lds0 + pc * 1024); else glds16(kv, lo, lds0 + 142 * 1024); }
    asm volatile("s_waitcnt vmcnt(16)\n\ts_barrier" : "+v"(qm), "+v"(qt) :: "memory");
    const int dd = r32 < 20 ? r32 : (r32 < 28 ? 20 : 19);
    const int voff = 2560 + (dd == 20 ? 640 : h * 320 + dd * 16);
    const unsigned ka = lds0 + lane * 16, ta = lds0 + 2048 + lane * 8, va = lds0 + voff;
    f32x16 negm;
    { const f32x16 zero = {};
      const f32x32 s = qk_step(lds_frag(ka), lds_frag(ka + 1024), lds_frag4(ta), qm, qt, zero);
      const float m0 = max32(s);
#pragma unroll
      for (int r = 0; r < 16; ++r) negm[r] = -m0; }
    f32x16 o = {};
    auto step = [&](unsigned off) {
        f32x32 s = qk_step(lds_frag(ka + off), lds_frag(ka + off + 1024), lds_frag4(ta + off), qm, qt, negm);
#pragma unroll
        for (int r = 0; r < 32; ++r) s[r] = __builtin_amdgcn_exp2f(s[r]);
#pragma unroll
        for (int j = 0; j < 4; ++j) o = MFMA32(lds_frag(va + off + j * 656), pack8(s, 8 * j), o);
    };
    for (int t = 0; t < 3; ++t) step(t * KV_STEP);
    asm volatile("s_waitcnt vmcnt(0)\n\ts_barrier" ::: "memory");
    for (int t = 3; t < 5; ++t) step(t * KV_STEP);
    bar_lgkm();
#pragma unroll
    for (int p = 0; p < 3; ++p) { const int pc = p * 8 + wid; if (pc < 21) glds16(kv + AT_RES * KV_STEP + pc * 1024, lo, lds0 + pc * 1024); }
    for (int t = 5; t < AT_RES; ++t) step(t * KV_STEP);
    asm volatile("s_waitcnt vmcnt(0)\n\ts_barrier" ::: "memory");
    for (int t = 0; t < 4; ++t) step(t * KV_STEP);
    bool bad = false;
#pragma unroll
    for (int r = 0; r < 8; ++r) bad |= !(fabsf(o[r]) < 1e30f);
    bad |= !(fabsf(o[8]) < 1e30f);
    if (h == 0) { bad |= !(fabsf(o[9]) < 1e30f) || !(fabsf(o[10]) < 1e30f) || !(fabsf(o[11]) < 1e30f); }
    if (__builtin_expect(__any(bad), 0)) o = attn_slow(kv, lane, voff, qm, qt);
    attn_store(o, lane, head, qb * 256 + wid * 32, ws);
}

constexpr int P_NS = 4, P_UNIT = 9 * 1024, P_SLOT = 4 * P_UNIT;
template <int NKB, bool FINAL, int NJ> DI void proj_compute(unsigned lds0, int wt, int j0, int lane, int m0, int n0, const u16* x, char* ws, u16* out) {
    constexpr int NST = NKB / 4;
    const int tk = lane & 15, g = lane >> 4;
    u32x2 res[NJ][2];
    size_t hoff[2];
#pragma unroll
    for (int i = 0; i < 2; ++i) { const int tok = m0 + 16 * (2 * wt + i) + tk;
        hoff[i] = HI_OFF + ((size_t)(tok >> 5) * 40 + (n0 >> 4) + j0) * 1024 + (g >> 1) * 512 + (tok & 31) * 16 + (g & 1) * 8;
#pragma unroll
        for (int j = 0; j < NJ; ++j) res[j][i] = FINAL ? *(const u32x2*)(ws + hoff[i] + j * 1024) : *(const u32x2*)(x + (size_t)tok * DM + n0 + 16 * (j0 + j) + 4 * g); }
    const unsigned ta = lds0 + lane * 16 + 2 * wt * 1024, wa = lds0 + lane * 16 + (4 + j0) * 1024;
    f32x4 acc[NJ][2] = {};
    bf16x8 tf[2][2], wf[2][NJ];
    auto load = [&](int buf, unsigned off) {
#pragma unroll
        for (int i = 0; i < 2; ++i) tf[buf][i] = lds_frag(ta + off + i * 1024);
#pragma unroll
        for (int j = 0; j < NJ; ++j) wf[buf][j] = lds_frag(wa + off + j * 1024);
    };
    auto mma = [&](int buf) {
#pragma unroll
        for (int j = 0; j < NJ; ++j)
#pragma unroll
            for (int i = 0; i < 2; ++i) acc[j][i] = MFMA16(wf[buf][j], tf[buf][i], acc[j][i]);
    };
    bar();
    load(0, 0);
    unsigned sl = 0;
    for (int s = 0; s < NST; ++s) {
        load(1, sl + P_UNIT); mma(0);
        load(0, sl + 2 * P_UNIT); mma(1);
        load(1, sl + 3 * P_UNIT); mma(0);
        sl += P_SLOT; if (sl == P_NS * P_SLOT) sl = 0;
        if (s < NST - 1) { bar_lgkm(); load(0, sl); }
        mma(1);
    }
#pragma unroll
    for (int i = 0; i < 2; ++i) { const int tok = m0 + 16 * (2 * wt + i) + tk;
#pragma unroll
        for (int j = 0; j < NJ; ++j) {
            const u32x2 w = {cvtpk(acc[j][i][0] + bf_lo(res[j][i][0]), acc[j][i][1] + bf_hi(res[j][i][0])), cvtpk(acc[j][i][2] + bf_lo(res[j][i][1]), acc[j][i][3] + bf_hi(res[j][i][1]))};
            if (FINAL) *(u32x2*)(out + (size_t)tok * DM + n0 + 16 * (j0 + j) + 4 * g) = w; else *(u32x2*)(ws + hoff[i] + j * 1024) = w;
        } }
}
template <int NKB, bool FINAL> __global__ __launch_bounds__(512, 2) void k_proj(const u16* x, char* ws, u16* out) {
    __shared__ __attribute__((aligned(16))) char smem[P_NS * P_SLOT];
    const int xcd = blockIdx.x & 7, idx = blockIdx.x >> 3;
    const int rt = xcd * 4 + (idx & 3), ct = idx >> 2, m0 = rt * 64, n0 = ct * 80;
    const int lane = threadIdx.x & 63, wid = __builtin_amdgcn_readfirstlane(threadIdx.x >> 6);
    const unsigned lds0 = (unsigned)(uintptr_t)smem;
    if (wid >= 4) {
        const int lw = wid - 4;
        const char* ab = ws + (FINAL ? GI_OFF : OI_OFF) + ((size_t)rt * 4 * NKB + lw) * 1024;
        const char* wb = ws + (FINAL ? WDT_OFF : WOT_OFF) + ((size_t)ct * 5 * NKB + lw) * 1024;
        const unsigned lo = lane * 16;
        loader_loop<NKB / 4, P_NS, 9>([&](int st, int slot) {
            const unsigned d = lds0 + slot * P_SLOT + lw * P_UNIT;
#pragma unroll
            for (int j = 0; j < 5; ++j) glds16(wb + (j * NKB + st * 4) * 1024, lo, d + (4 + j) * 1024);
#pragma unroll
            for (int i = 0; i < 4; ++i) glds16(ab + (i * NKB + st * 4) * 1024, lo, d + i * 1024);
        });
        return;
    }
    if (wid < 2) proj_compute<NKB, FINAL, 3>(lds0, wid & 1, 0, lane, m0, n0, x, ws, out);
    else proj_compute<NKB, FINAL, 2>(lds0, wid & 1, 3, lane, m0, n0, x, ws, out);
}

constexpr int G_NS = 4, G_UNIT = 10 * 1024, G_SLOT = 4 * G_UNIT, G_NST = 10;
__global__ __launch_bounds__(512, 2) void k_gateup(char* ws) {
    __shared__ __attribute__((aligned(16))) char smem[G_NS * G_SLOT];
    const int xcd = blockIdx.x & 7, idx = blockIdx.x >> 3;
    const int rt = (xcd >> 1) * 4 + (idx & 3), ct = (xcd & 1) * 8 + (idx >> 2);
    const int lane = threadIdx.x & 63, wid = __builtin_amdgcn_readfirstlane(threadIdx.x >> 6);
    const unsigned lds0 = (unsigned)(uintptr_t)smem;
    if (wid >= 4) {
        const int lw = wid - 4;
        const char* ab = ws + HI_OFF + ((size_t)rt * 4 * 40 + lw) * 1024;
        const char* wb = ws + WGU_OFF + ((size_t)ct * 6 * 40 + lw) * 1024;
        const unsigned lo = lane * 16;
        loader_loop<G_NST, G_NS, 10>([&](int st, int slot) {
            const unsigned d = lds0 + slot * G_SLOT + lw * G_UNIT;
#pragma unroll
            for (int j = 0; j < 6; ++j) glds16(wb + (j * 40 + st * 4) * 1024, lo, d + (4 + j) * 1024);
#pragma unroll
            for (int i = 0; i < 4; ++i) glds16(ab + (i * 40 + st * 4) * 1024, lo, d + i * 1024);
        });
        return;
    }
    const int wn = wid & 1, wt = wid >> 1, r32 = lane & 31, h = lane >> 5;
    const unsigned ta = lds0 + lane * 16 + (2 * wt + wn) * 1024, wa = lds0 + lane * 16 + (4 + 3 * wn + 2 * wt) * 1024;
    const int to1 = wn ? -1024 : 1024, wstep = wt ? -1024 : 1024;
    f32x16 acc[3][2] = {};
    float s1 = 0.f, s2 = 0.f, c0 = 0.f, c1 = 0.f;
    bf16x8 tf[2][2], wf[2][3];
    auto load = [&](int buf, unsigned off) {
        tf[buf][0] = lds_frag(ta + off); tf[buf][1] = lds_frag(ta + off + to1);
#pragma unroll
        for (int j = 0; j < 3; ++j) wf[buf][j] = lds_frag(wa + off + j * wstep);
    };
    auto mma = [&](int buf) {
#pragma unroll
        for (int j = 0; j < 3; ++j)
#pragma unroll
            for (int i = 0; i < 2; ++i) acc[j][i] = MFMA32(wf[buf][j], tf[buf][i], acc[j][i]);
        stat_sum(tf[buf][0], s1); stat_sq(tf[buf][0], s2); stat_sum(wf[buf][0], c0); stat_sum(wf[buf][1], c1);
    };
    bar();
    load(0, 0);
    unsigned sl = 0;
    for (int s = 0; s < G_NST; ++s) {
        load(1, sl + G_UNIT); mma(0);
        load(0, sl + 2 * G_UNIT); mma(1);
        load(1, sl + 3 * G_UNIT); mma(0);
        sl += G_SLOT; if (sl == G_NS * G_SLOT) sl = 0;
        if (s < G_NST - 1) { bar_lgkm(); load(0, sl); }
        mma(1);
    }
    LAS float* st = (LAS float*)(uintptr_t)(lds0 + 2 * G_SLOT);
    s1 = xhalf_add(s1); s2 = xhalf_add(s2); c0 = xhalf_add(c0); c1 = xhalf_add(c1);
    { const float mean = s1 * (1.f / DM); const float var = fmaxf(s2 * (1.f / DM) - mean * mean, 0.f) + LN_EPS;
      if (h == 0) { st[(2 * wt + wn) * 32 + r32] = mean; st[128 + (2 * wt + wn) * 32 + r32] = var; st[256 + (3 * wn + 2 * wt) * 32 + r32] = c0; if (wt == 0) st[256 + (3 * wn + 1) * 32 + r32] = c1; } }
    bar_lgkm();
    bf16x8 mu[2], cf[3];
#pragma unroll
    for (int i = 0; i < 2; ++i) mu[i] = split_b(-st[(2 * wt + (wn ^ i)) * 32 + r32], h);
#pragma unroll
    for (int j = 0; j < 3; ++j) cf[j] = split_a(st[256 + (3 * wn + (wt ? 2 - j : j)) * 32 + r32], h);
#pragma unroll
    for (int j = 0; j < 3; ++j)
#pragma unroll
        for (int i = 0; i < 2; ++i) acc[j][i] = MFMA32(cf[j], mu[i], acc[j][i]);
    constexpr float C0 = -2.f * 1.4426950408889634f * 0.7978845608028654f, C1 = C0 * 0.044715f;
#pragma unroll
    for (int i = 0; i < 2; ++i) {
        const int tl = (2 * wt + (wn ^ i)) * 32 + r32, tok = rt * 128 + tl;
        const float var = st[128 + tl], rs = __builtin_amdgcn_rsqf(var), sd = var * rs;
#pragma unroll
        for (int j = 0; j < 3; ++j) {
            const int nt = ct * 6 + 3 * wn + (wt ? 2 - j : j);
            float y[8];
#pragma unroll
            for (int r = 0; r < 8; ++r) { const float a = acc[j][i][r] * rs; const float e = __builtin_amdgcn_exp2f(a * (C0 + C1 * a * a));
                y[r] = a * acc[j][i][r + 8] * __builtin_amdgcn_rcpf(e * sd + sd); }
            const u32x4 w = {cvtpk(y[0], y[1]), cvtpk(y[2], y[3]), cvtpk(y[4], y[5]), cvtpk(y[6], y[7])};
            *(u32x4*)(ws + GI_OFF + ((size_t)(tok >> 4) * 48 + (nt >> 1)) * 1024 + (2 * (nt & 1) + h) * 256 + (tok & 15) * 16) = w;
        }
    }
}

}

extern "C" void kernel_launch(void* const* d_in, const int* in_sizes, int n_in, void* d_out, int out_size, void* d_ws, size_t ws_size, hipStream_t stream) {
    using namespace blk;
    (void)in_sizes; (void)n_in; (void)out_size; (void)ws_size;
    const u16* x = (const u16*)d_in[0]; const u16* wqkv = (const u16*)d_in[1]; const u16* wo = (const u16*)d_in[2];
    const u16* wg = (const u16*)d_in[4]; const u16* wu = (const u16*)d_in[5]; const u16* wd = (const u16*)d_in[6];
    char* ws = (char*)d_ws; u16* out = (u16*)d_out;
    k_retile<<<(RT_JOBS + 3) / 4, 256, 0, stream>>>(wo, wg, wu, wd, ws);
    k_qkv<<<256, 512, 0, stream>>>(x, wqkv, ws);
    k_attn<<<256, 512, 0, stream>>>(ws);
    k_proj<20, false><<<256, 512, 0, stream>>>(x, ws, out);
    k_gateup<<<256, 512, 0, stream>>>(ws);
    k_proj<48, true><<<256, 512, 0, stream>>>(x, ws, out);
}
```

```cpp
#include <hip/hip_runtime.h>
#include <hip/hip_bf16.h>
#include <stdint.h>

namespace blk {

typedef short bf16x8 __attribute__((ext_vector_type(8)));
typedef short bf16x4 __attribute__((ext_vector_type(4)));
typedef float f32x2 __attribute__((ext_vector_type(2)));
typedef float f32x4 __attribute__((ext_vector_type(4)));
typedef float f32x16 __attribute__((ext_vector_type(16)));
typedef float f32x32 __attribute__((ext_vector_type(32)));
typedef unsigned u32x2 __attribute__((ext_vector_type(2)));
typedef unsigned u32x4 __attribute__((ext_vector_type(4)));
typedef unsigned short u16;
typedef unsigned long long u64;
typedef __bf16 bfv2 __attribute__((ext_vector_type(2)));
#define LAS __attribute__((address_space(3)))
#define GAS __attribute__((address_space(1)))
#define DI __device__ __forceinline__

constexpr int SEQ = 2048, DM = 640, NH = 32, FF = 1536, NQKV = 1920;
constexpr float LN_EPS = 1e-5f;
constexpr float QSCALE = 0.22360679774997896f * 1.4426950408889634f;

constexpr int KV_STEP = 5120, KV_HEAD = 32 * KV_STEP, Q_HEAD = 128 * 640;
constexpr unsigned QT_OFF = 0;
constexpr unsigned KV_OFF = QT_OFF + NH * Q_HEAD;
constexpr unsigned OI_OFF = KV_OFF + NH * KV_HEAD;
constexpr unsigned HI_OFF = OI_OFF + 128 * 20 * 1024;
constexpr unsigned GI_OFF = HI_OFF + 64 * 40 * 1024;
constexpr unsigned ST2_OFF = GI_OFF + 128 * 48 * 1024;
constexpr unsigned WOT_OFF = ST2_OFF + 8 * 2048 * 8;
constexpr unsigned WGU_OFF = WOT_OFF + 40 * 20 * 1024;
constexpr unsigned WDT_OFF = WGU_OFF + 96 * 40 * 1024;
constexpr unsigned ST1_OFF = WDT_OFF + 40 * 48 * 1024;
constexpr unsigned CS1_OFF = ST1_OFF + 2048 * 16;
constexpr unsigned CF1_OFF = CS1_OFF + 16 * NQKV * 8;
constexpr unsigned CS2_OFF = CF1_OFF + 2048 * 8;
constexpr unsigned FLG_OFF = CS2_OFF + 256 * 1536;
#define F1_STRIDE 128
constexpr unsigned FR_OFF = FLG_OFF + 4096, F1_OFF = FLG_OFF + 8192;
constexpr unsigned WS_END = F1_OFF + 256 * 128;
constexpr unsigned MARK_SHARE = (WOT_OFF - OI_OFF) / 2048;
static_assert(MARK_SHARE == 5696 && MARK_SHARE * 2048 == WOT_OFF - OI_OFF, "");

template <int N> struct IC { static constexpr int v = N; };
template <int B, int E, class F> DI void static_for(F&& f) { if constexpr (B < E) { f(IC<B>{}); static_for<B + 1, E>(f); } }
template <int N> DI void wait_vm() { asm volatile("s_waitcnt vmcnt(%0)" :: "n"(N) : "memory"); }
DI void bar() { asm volatile("s_barrier" ::: "memory"); }
DI void bar_lgkm() { asm volatile("s_waitcnt lgkmcnt(0)\n\ts_barrier" ::: "memory"); }
DI void bar_drain() { asm volatile("s_waitcnt vmcnt(0) lgkmcnt(0)\n\ts_barrier" ::: "memory"); }
#define SB() __builtin_amdgcn_sched_barrier(0)
#define GL_(o, f) "global_load_lds_dwordx4 %0, %1 offset:" #o f "\n\t"
template <int N, bool DEV> DI void glds(const void* sbase, unsigned voff, unsigned lds_dst) {
    static_assert(N >= 1 && N <= 6, "");
    if constexpr (N <= 4) {
#define GLS_(f) do { if constexpr (N == 1) asm volatile("s_mov_b32 m0, %2\n\ts_nop 0\n\t" GL_(0, f) :: "v"(voff), "s"(sbase), "s"(lds_dst) : "memory"); \
        if constexpr (N == 4) asm volatile("s_mov_b32 m0, %2\n\ts_nop 0\n\t" GL_(0, f) GL_(1024, f) GL_(2048, f) GL_(3072, f) :: "v"(voff), "s"(sbase), "s"(lds_dst) : "memory"); } while (0)
        static_assert(N == 1 || N == 4, "");
        if (DEV) GLS_(" sc1"); else GLS_("");
    } else {
        if constexpr (N == 5) asm volatile("s_mov_b32 m0, %2\n\ts_nop 0\n\t" GL_(0, " sc1") GL_(1024, " sc1") GL_(2048, " sc1") GL_(3072, " sc1") "s_add_u32 m0, m0, 0x1000\n\ts_nop 0\n\tglobal_load_lds_dwordx4 %3, %1 sc1"
                                           :: "v"(voff), "s"(sbase), "s"(lds_dst), "v"(voff + 4096) : "memory", "scc");
        else asm volatile("s_mov_b32 m0, %2\n\ts_nop 0\n\t" GL_(0, " sc1") GL_(1024, " sc1") GL_(2048, " sc1") GL_(3072, " sc1") "s_add_u32 m0, m0, 0x1000\n\ts_nop 0\n\tglobal_load_lds_dwordx4 %3, %1 sc1\n\tglobal_load_lds_dwordx4 %3, %1 offset:1024 sc1"
                          :: "v"(voff), "s"(sbase), "s"(lds_dst), "v"(voff + 4096) : "memory", "scc");
    }
}
DI void touch(const void* sbase, unsigned voff, unsigned lds_dst) { asm volatile("s_mov_b32 m0, %2\n\ts_nop 0\n\tglobal_load_lds_dword %0, %1" :: "v"(voff), "s"(sbase), "s"(lds_dst) : "memory"); }
typedef __amdgpu_buffer_rsrc_t rsrc_t;
DI void st16(rsrc_t r, unsigned off, u32x4 v) { __builtin_amdgcn_raw_buffer_store_b128(v, r, off, 0, 16); }
DI void st8(rsrc_t r, unsigned off, u32x2 v) { __builtin_amdgcn_raw_buffer_store_b64(v, r, off, 0, 16); }
DI u32x4 ld16(rsrc_t r, unsigned off, unsigned soff = 0) { return __builtin_amdgcn_raw_buffer_load_b128(r, off, soff, 16); }
DI u32x2 ld8(rsrc_t r, unsigned off) { return __builtin_amdgcn_raw_buffer_load_b64(r, off, 0, 16); }
DI unsigned ld4(rsrc_t r, unsigned off) { return __builtin_amdgcn_raw_buffer_load_b32(r, off, 0, 16); }
DI unsigned cvtpk(float lo, float hi) { const f32x2 v = {lo, hi}; return __builtin_bit_cast(unsigned, __builtin_convertvector(v, bfv2)); }
DI float bf_lo(unsigned w) { return __uint_as_float(w << 16); }
DI float bf_hi(unsigned w) { return __uint_as_float(w & 0xffff0000u); }
DI bf16x8 lds_frag(unsigned addr) { return *(const LAS bf16x8*)(uintptr_t)addr; }
DI bf16x4 lds_tr(unsigned addr) { return __builtin_amdgcn_ds_read_tr16_b64_v4i16((LAS bf16x4*)(uintptr_t)addr); }
DI bf16x8 cat(bf16x4 a, bf16x4 b) { return __builtin_shufflevector(a, b, 0, 1, 2, 3, 4, 5, 6, 7); }
#define MFMA32(a, b, c) __builtin_amdgcn_mfma_f32_32x32x16_bf16((a), (b), (c), 0, 0, 0)
#define MFMA16(a, b, c) __builtin_amdgcn_mfma_f32_16x16x32_bf16((a), (b), (c), 0, 0, 0)
#define MFMA2B(a, b, c) __builtin_amdgcn_mfma_f32_32x32x4bf16_1k((a), (b), (c), 0, 0, 0)
DI bf16x8 split_a(float a, int h) { const unsigned hi = cvtpk(a, a); const float lo = a - bf_lo(hi); u32x4 w = {hi, cvtpk(lo, 0.f), 0u, 0u}; if (h) w = (u32x4){0u, 0u, 0u, 0u}; return __builtin_bit_cast(bf16x8, w); }
DI bf16x8 split_b(float b, int h) { const unsigned hi = cvtpk(b, 0.f); const float lo = b - bf_lo(hi); u32x4 w = {cvtpk(b, lo), hi, 0u, 0u}; if (h) w = (u32x4){0u, 0u, 0u, 0u}; return __builtin_bit_cast(bf16x8, w); }

template <int CTRL> DI float dpp_add(float v) { return v + __uint_as_float(__builtin_amdgcn_update_dpp(0u, __float_as_uint(v), CTRL, 0xf, 0xf, true)); }
DI float sum16(float v) { v = dpp_add<0xB1>(v); v = dpp_add<0x4E>(v); v = dpp_add<0x141>(v); return dpp_add<0x140>(v); }
DI float add_x16(float v) { const auto r = __builtin_amdgcn_permlane16_swap(__float_as_uint(v), __float_as_uint(v), false, false); return __uint_as_float(r[0]) + __uint_as_float(r[1]); }
DI float add_x32(float v) { const auto r = __builtin_amdgcn_permlane32_swap(__float_as_uint(v), __float_as_uint(v), false, false); return __uint_as_float(r[0]) + __uint_as_float(r[1]); }
DI float max_x32(float v) { const auto r = __builtin_amdgcn_permlane32_swap(__float_as_uint(v), __float_as_uint(v), false, false); return fmaxf(__uint_as_float(r[0]), __uint_as_float(r[1])); }

DI unsigned nomark(unsigned v) { return v < 0xFFFFFFFEu ? v : 0xFFFFFFFEu; }
DI u32x4 nomark4(u32x4 v) { return (u32x4){nomark(v[0]), nomark(v[1]), nomark(v[2]), nomark(v[3])}; }
DI unsigned umax3(unsigned a, unsigned b, unsigned c) { const unsigned m = a > b ? a : b; return m > c ? m : c; }
#define REDO_(c) __builtin_expect(c, 0)
DI bool marked(u32x4 v) { return umax3(umax3(v[0], v[1], v[2]), v[3], 0u) == 0xFFFFFFFFu; }

constexpr int SPIN_MAX = 60000;
DI void st_flag(char* ws, unsigned off, u64 v) { __hip_atomic_store((GAS u64*)(ws + off), v, __ATOMIC_RELAXED, __HIP_MEMORY_SCOPE_AGENT); }
#define POLL_SLEEP 2
DI void poll_mark(rsrc_t wr, unsigned o) {
#pragma unroll 1
    for (int spin = 0; spin < SPIN_MAX; ++spin) { if (__all(ld4(wr, o) != 0xFFFFFFFFu)) break; asm volatile("s_sleep %0" :: "n"(POLL_SLEEP) : "memory"); }
    asm volatile("" ::: "memory");
}
DI void settle(rsrc_t wr, u32x4& v, unsigned off, unsigned soff) {
    if (__builtin_expect(__any(marked(v)), 0)) {
#pragma unroll 1
        for (int spin = 0; spin < SPIN_MAX; ++spin) { asm volatile("s_sleep 1" ::: "memory"); v = ld16(wr, off, soff); if (!__any(marked(v))) break; }
    }
}

DI u64 uni64(u32x2 g) { return (u64)__builtin_amdgcn_readfirstlane(g[1]) << 32 | __builtin_amdgcn_readfirstlane(g[0]); }

struct RtJob { unsigned src, src2, dst, dst2; };
template <bool GU> DI RtJob retile_decode(int w, int lane, int& sel) {
    sel = w >= 960; const bool ok = w < (GU ? 1920 : 1360); w = ok ? w - 960 * sel : 0;
    constexpr int NB = GU ? 12 : 5, TG = GU ? 6 : 5, TS = GU ? 2048 : 1024, ld = GU ? FF : DM;
    const int ci = w / NB, nb = w - ci * NB, p = ci / 5, r = ci - p * 5;
    const int ra = GU || !sel ? 8 * ci : r < 4 ? 20 * (2 * p + (r >> 1)) + 4 * (r & 1) : 40 * p + 16, rb = GU || !sel ? 4 : r == 4 ? 20 : 8;
    const unsigned src = (ra * ld + nb * 128 + 2 * lane) * 2, gb = (!GU && sel ? 100 : 240) * 1024;
    const unsigned base = (GU ? WGU_OFF + (ci >> 2) * 12288 + sel * 1024 : (sel ? WOT_OFF : WDT_OFF) + (ci >> 2) * 5120) + (ci & 3) * 256 + (2 * (lane & 7) + ((lane >> 3) & 1)) * 16;
    const unsigned nt = nb * 8 + 2 * (lane >> 4), g0 = nt / TG, g1 = (nt + 1) / TG;
    return RtJob{src, src + rb * ld * 2, ok ? base + g0 * gb + (nt - g0 * TG) * TS : 0xF0000000u, ok ? base + g1 * gb + (nt + 1 - g1 * TG) * TS : 0xF0000000u};
}
DI float rbf(float v) { return bf_lo(cvtpk(v, 0.f)); }
DI void ln_finish(float mean, float var, float& mu, float& sd) { mu = rbf(mean); sd = rbf(__builtin_amdgcn_sqrtf(rbf(rbf(var) + LN_EPS))); }
DI void row_stats(const u32x2 (&v)[5], int row0, int lane, rsrc_t wr, unsigned tag32) {
    f32x2 e[10], s = {};
#pragma unroll
    for (int i = 0; i < 10; ++i) { e[i] = (f32x2){bf_lo(v[i >> 1][i & 1]), bf_hi(v[i >> 1][i & 1])}; s += e[i]; }
    const float mean = add_x16(sum16(s[0] + s[1])) * (1.f / DM); f32x2 q = {};
#pragma unroll
    for (int i = 0; i < 10; ++i) { const f32x2 d = e[i] - mean; q += d * d; }
    float mu, sd; ln_finish(mean, add_x16(sum16(q[0] + q[1])) * (1.f / DM), mu, sd);
    if ((lane & 31) == 0) st16(wr, ST1_OFF + (row0 + (lane >> 5)) * 16, (u32x4){__float_as_uint(mu), tag32, __float_as_uint(1.f / sd), tag32});
}

#define P1_DEPTH 2
constexpr int P1_SLOT = 32768, P1_RL = 4 * P1_SLOT;
DI void p1_compute(unsigned lds0, int wid, int lane, int m0, int ct, rsrc_t wr, unsigned tag32) {
    const int wrow = wid >> 1, wc = wid & 1, r32 = lane & 31, h = lane >> 5;
    unsigned xa[2][4], wa[2][2];
    { const int row = wrow * 64 + r32, G = lane >> 4, q = (lane & 15) >> 2, p = lane & 3;
#pragma unroll
      for (int b = 0; b < 2; ++b) {
#pragma unroll
          for (int ks = 0; ks < 4; ++ks) { xa[b][ks] = lds0 + b * 2 * P1_SLOT + row * 128 + (((2 * ks + h) ^ ((row >> 1) & 7)) << 4); asm volatile("" : "+v"(xa[b][ks])); }
#pragma unroll
          for (int t = 0; t < 2; ++t) { const int c = wc * 8 + t * 4 + (G & 1) * 2 + (p >> 1);
              wa[b][t] = lds0 + b * 2 * P1_SLOT + 16384 + (8 * (G >> 1) + q) * 256 + ((c ^ (q << 2)) << 4) + (p & 1) * 8; asm volatile("" : "+v"(wa[b][t])); } } }
    constexpr int D = P1_DEPTH;
    f32x16 acc[2][2] = {};
    bf16x8 xf[D + 1][2]; bf16x4 wl[D + 1][2], wh[D + 1][2];
    u32x4 st[2]; u32x2 cs[2];
    const unsigned sto = ST1_OFF + (m0 + wrow * 64 + r32) * 16, cso = CF1_OFF + (ct * 128 + wc * 64 + r32) * 8;
    auto rdx = [&](int buf, int k, int i) { xf[buf][i] = lds_frag(xa[(k >> 3) & 1][k & 3] + ((k >> 2) & 1) * P1_SLOT + i * 4096); };
    auto rdw = [&](int buf, int k, int t) { const unsigned a = wa[(k >> 3) & 1][t] + ((k >> 2) & 1) * P1_SLOT + (k & 3) * 4096; wl[buf][t] = lds_tr(a); wh[buf][t] = lds_tr(a + 1024); };
    bar();
#pragma unroll
    for (int d = 0; d < D; ++d) { rdx(d, d, 0); rdw(d, d, 0); rdx(d, d, 1); rdw(d, d, 1); }
    static_for<0, 40>([&](auto kk) {
        constexpr int k = kk.v, c = k % (D + 1), kn = k + D, n = kn % (D + 1);
        constexpr bool more = kn < 40, edge = (kn & 3) == 0 && more;
        SB();
        acc[0][0] = MFMA32(xf[c][0], cat(wl[c][0], wh[c][0]), acc[0][0]); if (more && !edge) rdx(n, kn, 0); SB();
        acc[1][0] = MFMA32(xf[c][1], cat(wl[c][0], wh[c][0]), acc[1][0]); if (more && !edge) rdw(n, kn, 0); SB();
        if (edge) { bar_lgkm(); rdx(n, kn, 0); rdw(n, kn, 0); SB(); }
        acc[0][1] = MFMA32(xf[c][0], cat(wl[c][1], wh[c][1]), acc[0][1]); if (more) rdx(n, kn, 1); SB();
        acc[1][1] = MFMA32(xf[c][1], cat(wl[c][1], wh[c][1]), acc[1][1]); if (more) rdw(n, kn, 1); SB();
        if (k == 29) { st[0] = ld16(wr, sto); SB(); } if (k == 31) { st[1] = ld16(wr, sto + 512); SB(); }
        if (k == 33) { cs[0] = ld8(wr, cso); SB(); } if (k == 35) { cs[1] = ld8(wr, cso + 256); SB(); }
    });
#pragma unroll 1
    for (int spin = 0; spin < SPIN_MAX; ++spin) {
        if (__all(((st[0][1] ^ tag32) | (st[0][3] ^ tag32) | (st[1][1] ^ tag32) | (st[1][3] ^ tag32) | (cs[0][1] ^ tag32) | (cs[1][1] ^ tag32)) == 0)) break;
        asm volatile("" ::: "memory");
        st[0] = ld16(wr, sto); st[1] = ld16(wr, sto + 512); cs[0] = ld8(wr, cso); cs[1] = ld8(wr, cso + 256); }
    LAS float* rl = (LAS float*)(uintptr_t)(lds0 + P1_RL) + wid * 64;
    rl[lane] = __uint_as_float(h ? st[1][2] : st[0][2]) * (ct < 5 ? QSCALE : 1.f);
#pragma unroll
    for (int i = 0; i < 2; ++i)
#pragma unroll
        for (int t = 0; t < 2; ++t) acc[i][t] = MFMA32(split_b(-__uint_as_float(st[i][0]), h), split_a(__uint_as_float(cs[t][0]), h), acc[i][t]);
    const int sec = ct / 5;
#pragma unroll
    for (int t = 0; t < 2; ++t) {
        const int col = (ct - sec * 5) * 128 + wc * 64 + t * 32 + r32, head = col / 20, d = col - head * 20;
        const int g0 = (m0 + wrow * 64) >> 4;
        const unsigned base = (sec == 0 ? QT_OFF + head * Q_HEAD + g0 * 640 : KV_OFF + head * KV_HEAD + (g0 >> 2) * KV_STEP + (sec - 1) * 2560) + h * 320 + d * 16;
#pragma unroll
        for (int i = 0; i < 2; ++i)
#pragma unroll
            for (int s = 0; s < 2; ++s) {
                const f32x4 ra = *(const LAS f32x4*)(rl + i * 32 + 16 * s + 4 * h), rb = *(const LAS f32x4*)(rl + i * 32 + 16 * s + 8 + 4 * h);
                const f32x16& a = acc[i][t];
                const u32x4 w = {cvtpk(a[8 * s] * ra[0], a[8 * s + 1] * ra[1]), cvtpk(a[8 * s + 2] * ra[2], a[8 * s + 3] * ra[3]),
                                 cvtpk(a[8 * s + 4] * rb[0], a[8 * s + 5] * rb[1]), cvtpk(a[8 * s + 6] * rb[2], a[8 * s + 7] * rb[3])};
                st16(wr, base + (2 * i + s) * 640, w);
            }
    }
}
DI void p1_loader(unsigned lds0, int lw, int lane, int rt, int ct, const u16* x, const u16* wqkv) {
    unsigned xo[4], wof[4];
#pragma unroll
    for (int p = 0; p < 4; ++p) {
        const int row = (4 * lw + p) * 8 + (lane >> 3); xo[p] = row * (DM * 2) + (((lane & 7) ^ ((row >> 1) & 7)) << 4);
        const int kr = (4 * lw + p) * 4 + (lane >> 4); wof[p] = kr * (NQKV * 2) + (((lane & 15) ^ ((kr & 3) << 2)) << 4);
    }
    int kt = ((ct & 7) + 3 * (rt & 3)) % 10;
    const unsigned dx = lds0 + 4 * lw * 1024;
    const char* xb = (const char*)x + rt * (128 * DM * 2) + kt * 128; const char* wb = (const char*)wqkv + kt * (64 * NQKV * 2) + ct * 256;
    auto issue = [&](auto ss) {
        constexpr int so = (ss.v & 3) * P1_SLOT;
#define P1L_(v, b) "s_add_u32 m0, m0, 0x400\n\ts_nop 0\n\tglobal_load_lds_dwordx4 " v ", " b "\n\t"
        asm volatile("s_add_u32 m0, %10, %12\n\ts_nop 0\n\tglobal_load_lds_dwordx4 %0, %8\n\t" P1L_("%1", "%8") P1L_("%2", "%8") P1L_("%3", "%8")
                     "s_add_u32 m0, %10, %11\n\ts_nop 0\n\tglobal_load_lds_dwordx4 %4, %9\n\t" P1L_("%5", "%9") P1L_("%6", "%9") P1L_("%7", "%9")
                     :: "v"(wof[0]), "v"(wof[1]), "v"(wof[2]), "v"(wof[3]), "v"(xo[0]), "v"(xo[1]), "v"(xo[2]), "v"(xo[3]), "s"(wb), "s"(xb), "s"(dx), "n"(so), "n"(so + 16384) : "memory", "scc");
        const bool wrap = kt == 9; kt = wrap ? 0 : kt + 1;
        xb += wrap ? -9 * 128 : 128; wb += wrap ? -9 * 64 * NQKV * 2 : 64 * NQKV * 2;
        asm volatile("" : "+s"(xb), "+s"(wb), "+s"(kt));
    };
    issue(IC<0>{}); issue(IC<1>{}); issue(IC<2>{});
    wait_vm<16>(); bar();
    static_for<0, 7>([&](auto ss) { issue(IC<ss.v + 3>{}); wait_vm<16>(); bar(); });
    wait_vm<8>(); bar();
    wait_vm<0>(); bar();
}

#define ATTN_ASM_BODY \
    "s_mov_b32 m0, %[ldsd]\n\t" \
    "v_mov_b32 v160, %[q0]\n\t" \
    "v_mov_b32 v161, %[q1]\n\t" \
    "v_mov_b32 v162, %[q2]\n\t" \
    "v_mov_b32 v163, %[q3]\n\t" \
    "v_mov_b32 v164, %[q4]\n\t" \
    "v_mov_b32 v165, %[q5]\n\t" \
    "v_mov_b32 v200, %[ka]\n\t" \
    "v_mov_b32 v201, %[ta]\n\t" \
    "v_mov_b32 v202, %[va]\n\t" \
    "v_mov_b32 v203, %[lo]\n\t" \
    "ds_read_b64_tr_b16 v[166:167], v200 offset:0\n\t" \
    "ds_read_b64_tr_b16 v[168:169], v200 offset:64\n\t" \
    "ds_read_b64_tr_b16 v[170:171], v200 offset:1280\n\t" \
    "ds_read_b64_tr_b16 v[172:173], v200 offset:1344\n\t" \
    "ds_read_b64_tr_b16 v[174:175], v201 offset:0\n\t" \
    "v_mov_b32 v144, %[negm]\n\t" \
    "v_mov_b32 v145, %[negm]\n\t" \
    "v_mov_b32 v146, %[negm]\n\t" \
    "v_mov_b32 v147, %[negm]\n\t" \
    "v_mov_b32 v148, %[negm]\n\t" \
    "v_mov_b32 v149, %[negm]\n\t" \
    "v_mov_b32 v150, %[negm]\n\t" \
    "v_mov_b32 v151, %[negm]\n\t" \
    "v_mov_b32 v152, %[negm]\n\t" \
    "v_mov_b32 v153, %[negm]\n\t" \
    "v_mov_b32 v154, %[negm]\n\t" \
    "v_mov_b32 v155, %[negm]\n\t" \
    "v_mov_b32 v156, %[negm]\n\t" \
    "v_mov_b32 v157, %[negm]\n\t" \
    "v_mov_b32 v158, %[negm]\n\t" \
    "v_mov_b32 v159, %[negm]\n\t" \
    "v_mov_b32 v128, 0\n\t" \
    "v_mov_b32 v129, 0\n\t" \
    "v_mov_b32 v130, 0\n\t" \
    "v_mov_b32 v131, 0\n\t" \
    "v_mov_b32 v132, 0\n\t" \
    "v_mov_b32 v133, 0\n\t" \
    "v_mov_b32 v134, 0\n\t" \
    "v_mov_b32 v135, 0\n\t" \
    "v_mov_b32 v136, 0\n\t" \
    "v_mov_b32 v137, 0\n\t" \
    "v_mov_b32 v138, 0\n\t" \
    "v_mov_b32 v139, 0\n\t" \
    "v_mov_b32 v140, 0\n\t" \
    "v_mov_b32 v141, 0\n\t" \
    "v_mov_b32 v142, 0\n\t" \
    "v_mov_b32 v143, 0\n\t" \
    "v_mov_b32 v176, %[vinit]\n\t" \
    "v_mov_b32 v177, %[vinit]\n\t" \
    "v_mov_b32 v178, %[vinit]\n\t" \
    "v_mov_b32 v179, %[vinit]\n\t" \
    "v_mov_b32 v180, %[vinit]\n\t" \
    "v_mov_b32 v181, %[vinit]\n\t" \
    "v_mov_b32 v182, %[vinit]\n\t" \
    "v_mov_b32 v183, %[vinit]\n\t" \
    "v_mov_b32 v184, %[vinit]\n\t" \
    "v_mov_b32 v185, %[vinit]\n\t" \
    "v_mov_b32 v186, %[vinit]\n\t" \
    "v_mov_b32 v187, %[vinit]\n\t" \
    "v_mov_b32 v188, %[vinit]\n\t" \
    "v_mov_b32 v189, %[vinit]\n\t" \
    "v_mov_b32 v190, %[vinit]\n\t" \
    "v_mov_b32 v191, %[vinit]\n\t" \
    "v_mov_b32 v196, 0\n\t" \
    "v_mov_b32 v197, 0\n\t" \
    "v_mov_b32 v198, 0\n\t" \
    "v_mov_b32 v199, 0\n\t" \
    "s_waitcnt lgkmcnt(0)\n\t" \
    "v_mfma_f32_32x32x16_bf16 v[64:79], v[166:169], v[160:163], v[144:159]\n\t" \
    "v_mfma_f32_32x32x16_bf16 v[80:95], v[170:173], v[160:163], v[144:159]\n\t" \
    "ds_read_b64_tr_b16 v[166:167], v200 offset:5120\n\t" \
    "ds_read_b64_tr_b16 v[168:169], v200 offset:5184\n\t" \
    "ds_read_b64_tr_b16 v[170:171], v200 offset:6400\n\t" \
    "ds_read_b64_tr_b16 v[172:173], v200 offset:6464\n\t" \
    "s_nop 5\n\t" \
    "v_mfma_f32_32x32x4_2b_bf16 v[64:95], v[174:175], v[164:165], v[64:95]\n\t" \
    "ds_read_b64_tr_b16 v[174:175], v201 offset:5120\n\t" \
    "s_nop 15\n\t" \
    "s_nop 3\n\t" \
    "s_setprio 3\n\t" \
    "s_waitcnt lgkmcnt(0)\n\t" \
    "v_mfma_f32_32x32x16_bf16 v[96:111], v[166:169], v[160:163], v[144:159]\n\t" \
    "ds_read_b64_tr_b16 v[166:167], v200 offset:10240\n\t" \
    "v_exp_f32 v64, v64\n\t" \
    "ds_read_b64_tr_b16 v[168:169], v200 offset:10304\n\t" \
    "v_exp_f32 v65, v65\n\t" \
    "v_mfma_f32_32x32x16_bf16 v[128:143], v[188:191], v[196:199], v[128:143]\n\t" \
    "s_mov_b64 exec, %[vmask]\n\t" \
    "ds_read_b128 v[176:179], v202 offset:0\n\t" \
    "ds_read_b128 v[180:183], v202 offset:640\n\t" \
    "ds_read_b128 v[184:187], v202 offset:1280\n\t" \
    "ds_read_b128 v[188:191], v202 offset:1920\n\t" \
    "s_mov_b64 exec, -1\n\t" \
    "v_exp_f32 v66, v66\n\t" \
    "v_exp_f32 v67, v67\n\t" \
    "v_exp_f32 v68, v68\n\t" \
    "v_mfma_f32_32x32x16_bf16 v[112:127], v[170:173], v[160:163], v[144:159]\n\t" \
    "ds_read_b64_tr_b16 v[170:171], v200 offset:11520\n\t" \
    "v_exp_f32 v69, v69\n\t" \
    "ds_read_b64_tr_b16 v[172:173], v200 offset:11584\n\t" \
    "v_exp_f32 v70, v70\n\t" \
    "s_add_u32 m0, m0, 0x2000\n\t" \
    "v_add_u32 v203, 0x2000, v203\n\t" \
    "global_load_lds_dwordx4 v203, %[kv] sc1\n\t" \
    "v_exp_f32 v71, v71\n\t" \
    "v_cvt_pk_bf16_f32 v192, v64, v65\n\t" \
    "v_cvt_pk_bf16_f32 v193, v66, v67\n\t" \
    "v_cvt_pk_bf16_f32 v194, v68, v69\n\t" \
    "v_cvt_pk_bf16_f32 v195, v70, v71\n\t" \
    "v_exp_f32 v72, v72\n\t" \
    "v_exp_f32 v73, v73\n\t" \
    "s_add_u32 m0, m0, 0x2000\n\t" \
    "v_add_u32 v203, 0x2000, v203\n\t" \
    "global_load_lds_dwordx4 v203, %[kv] sc1\n\t" \
    "v_exp_f32 v74, v74\n\t" \
    "v_exp_f32 v75, v75\n\t" \
    "v_mfma_f32_32x32x4_2b_bf16 v[96:127], v[174:175], v[164:165], v[96:127]\n\t" \
    "ds_read_b64_tr_b16 v[174:175], v201 offset:10240\n\t" \
    "v_exp_f32 v76, v76\n\t" \
    "v_exp_f32 v77, v77\n\t" \
    "s_add_u32 m0, m0, 0x2000\n\t" \
    "v_add_u32 v203, 0x2000, v203\n\t" \
    "global_load_lds_dwordx4 v203, %[kv] sc1\n\t" \
    "v_exp_f32 v78, v78\n\t" \
    "v_exp_f32 v79, v79\n\t" \
    "v_cvt_pk_bf16_f32 v196, v72, v73\n\t" \
    "v_cvt_pk_bf16_f32 v197, v74, v75\n\t" \
    "v_cvt_pk_bf16_f32 v198, v76, v77\n\t" \
    "v_cvt_pk_bf16_f32 v199, v78, v79\n\t" \
    "v_exp_f32 v80, v80\n\t" \
    "s_setprio 2\n\t" \
    "s_waitcnt lgkmcnt(3)\n\t" \
    "v_exp_f32 v81, v81\n\t" \
    "v_mfma_f32_32x32x16_bf16 v[128:143], v[176:179], v[192:195], v[128:143]\n\t" \
    "v_exp_f32 v82, v82\n\t" \
    "v_exp_f32 v83, v83\n\t" \
    "s_add_u32 m0, m0, 0x2000\n\t" \
    "v_add_u32 v203, 0x2000, v203\n\t" \
    "global_load_lds_dwordx4 v203, %[kv] sc1\n\t" \
    "v_exp_f32 v84, v84\n\t" \
    "v_exp_f32 v85, v85\n\t" \
    "v_mfma_f32_32x32x16_bf16 v[128:143], v[180:183], v[196:199], v[128:143]\n\t" \
    "v_exp_f32 v86, v86\n\t" \
    "v_exp_f32 v87, v87\n\t" \
    "v_cvt_pk_bf16_f32 v192, v80, v81\n\t" \
    "v_cvt_pk_bf16_f32 v193, v82, v83\n\t" \
    "v_cvt_pk_bf16_f32 v194, v84, v85\n\t" \
    "v_cvt_pk_bf16_f32 v195, v86, v87\n\t" \
    "v_exp_f32 v88, v88\n\t" \
    "v_exp_f32 v89, v89\n\t" \
    "v_mfma_f32_32x32x16_bf16 v[128:143], v[184:187], v[192:195], v[128:143]\n\t" \
    "v_exp_f32 v90, v90\n\t" \
    "v_exp_f32 v91, v91\n\t" \
    "s_add_u32 m0, m0, 0x2000\n\t" \
    "v_add_u32 v203, 0x2000, v203\n\t" \
    "global_load_lds_dwordx4 v203, %[kv] sc1\n\t" \
    "v_exp_f32 v92, v92\n\t" \
    "v_exp_f32 v93, v93\n\t" \
    "s_add_u32 m0, m0, 0x2000\n\t" \
    "v_add_u32 v203, 0x2000, v203\n\t" \
    "global_load_lds_dwordx4 v203, %[kv] sc1\n\t" \
    "v_exp_f32 v94, v94\n\t" \
    "v_exp_f32 v95, v95\n\t" \
    "v_cvt_pk_bf16_f32 v196, v88, v89\n\t" \
    "v_cvt_pk_bf16_f32 v197, v90, v91\n\t" \
    "v_cvt_pk_bf16_f32 v198, v92, v93\n\t" \
    "v_cvt_pk_bf16_f32 v199, v94, v95\n\t" \
    "s_setprio 1\n\t" \
    "s_waitcnt lgkmcnt(0)\n\t" \
    "v_mfma_f32_32x32x16_bf16 v[64:79], v[166:169], v[160:163], v[144:159]\n\t" \
    "ds_read_b64_tr_b16 v[166:167], v200 offset:15360\n\t" \
    "v_exp_f32 v96, v96\n\t" \
    "ds_read_b64_tr_b16 v[168:169], v200 offset:15424\n\t" \
    "v_exp_f32 v97, v97\n\t" \
    "v_mfma_f32_32x32x16_bf16 v[128:143], v[188:191], v[196:199], v[128:143]\n\t" \
    "s_mov_b64 exec, %[vmask]\n\t" \
    "ds_read_b128 v[176:179], v202 offset:5120\n\t" \
    "ds_read_b128 v[180:183], v202 offset:5760\n\t" \
    "ds_read_b128 v[184:187], v202 offset:6400\n\t" \
    "ds_read_b128 v[188:191], v202 offset:7040\n\t" \
    "s_mov_b64 exec, -1\n\t" \
    "v_exp_f32 v98, v98\n\t" \
    "v_exp_f32 v99, v99\n\t" \
    "v_exp_f32 v100, v100\n\t" \
    "v_mfma_f32_32x32x16_bf16 v[80:95], v[170:173], v[160:163], v[144:159]\n\t" \
    "ds_read_b64_tr_b16 v[170:171], v200 offset:16640\n\t" \
    "v_exp_f32 v101, v101\n\t" \
    "ds_read_b64_tr_b16 v[172:173], v200 offset:16704\n\t" \
    "v_exp_f32 v102, v102\n\t" \
    "s_add_u32 m0, m0, 0x2000\n\t" \
    "v_add_u32 v203, 0x2000, v203\n\t" \
    "global_load_lds_dwordx4 v203, %[kv] sc1\n\t" \
    "v_exp_f32 v103, v103\n\t" \
    "v_cvt_pk_bf16_f32 v192, v96, v97\n\t" \
    "v_cvt_pk_bf16_f32 v193, v98, v99\n\t" \
    "v_cvt_pk_bf16_f32 v194, v100, v101\n\t" \
    "v_cvt_pk_bf16_f32 v195, v102, v103\n\t" \
    "v_exp_f32 v104, v104\n\t" \
    "v_exp_f32 v105, v105\n\t" \
    "v_exp_f32 v106, v106\n\t" \
    "v_exp_f32 v107, v107\n\t" \
    "v_mfma_f32_32x32x4_2b_bf16 v[64:95], v[174:175], v[164:165], v[64:95]\n\t" \
    "ds_read_b64_tr_b16 v[174:175], v201 offset:15360\n\t" \
    "v_exp_f32 v108, v108\n\t" \
    "v_exp_f32 v109, v109\n\t" \
    "s_add_u32 m0, m0, 0x2000\n\t" \
    "v_add_u32 v203, 0x2000, v203\n\t" \
    "global_load_lds_dwordx4 v203, %[kv] sc1\n\t" \
    "v_exp_f32 v110, v110\n\t" \
    "v_exp_f32 v111, v111\n\t" \
    "v_cvt_pk_bf16_f32 v196, v104, v105\n\t" \
    "v_cvt_pk_bf16_f32 v197, v106, v107\n\t" \
    "v_cvt_pk_bf16_f32 v198, v108, v109\n\t" \
    "v_cvt_pk_bf16_f32 v199, v110, v111\n\t" \
    "v_exp_f32 v112, v112\n\t" \
    "s_setprio 0\n\t" \
    "s_waitcnt lgkmcnt(3)\n\t" \
    "v_exp_f32 v113, v113\n\t" \
    "v_mfma_f32_32x32x16_bf16 v[128:143], v[176:179], v[192:195], v[128:143]\n\t" \
    "v_exp_f32 v114, v114\n\t" \
    "v_exp_f32 v115, v115\n\t" \
    "s_add_u32 m0, m0, 0x2000\n\t" \
    "v_add_u32 v203, 0x2000, v203\n\t" \
    "global_load_lds_dwordx4 v203, %[kv] sc1\n\t" \
    "v_exp_f32 v116, v116\n\t" \
    "v_exp_f32 v117, v117\n\t" \
    "v_mfma_f32_32x32x16_bf16 v[128:143], v[180:183], v[196:199], v[128:143]\n\t" \
    "v_exp_f32 v118, v118\n\t" \
    "v_exp_f32 v119, v119\n\t" \
    "v_cvt_pk_bf16_f32 v192, v112, v113\n\t" \
    "v_cvt_pk_bf16_f32 v193, v114, v115\n\t" \
    "v_cvt_pk_bf16_f32 v194, v116, v117\n\t" \
    "v_cvt_pk_bf16_f32 v195, v118, v119\n\t" \
    "v_exp_f32 v120, v120\n\t" \
    "v_exp_f32 v121, v121\n\t" \
    "v_mfma_f32_32x32x16_bf16 v[128:143], v[184:187], v[192:195], v[128:143]\n\t" \
    "v_exp_f32 v122, v122\n\t" \
    "v_exp_f32 v123, v123\n\t" \
    "s_add_u32 m0, m0, 0x2000\n\t" \
    "v_add_u32 v203, 0x2000, v203\n\t" \
    "global_load_lds_dwordx4 v203, %[kv] sc1\n\t" \
    "v_exp_f32 v124, v124\n\t" \
    "v_exp_f32 v125, v125\n\t" \
    "s_add_u32 m0, m0, 0x2000\n\t" \
    "v_add_u32 v203, 0x2000, v203\n\t" \
    "global_load_lds_dwordx4 v203, %[kv] sc1\n\t" \
    "v_exp_f32 v126, v126\n\t" \
    "v_exp_f32 v127, v127\n\t" \
    "v_cvt_pk_bf16_f32 v196, v120, v121\n\t" \
    "v_cvt_pk_bf16_f32 v197, v122, v123\n\t" \
    "v_cvt_pk_bf16_f32 v198, v124, v125\n\t" \
    "v_cvt_pk_bf16_f32 v199, v126, v127\n\t" \
    "s_setprio 3\n\t" \
    "s_waitcnt lgkmcnt(0)\n\t" \
    "v_mfma_f32_32x32x16_bf16 v[96:111], v[166:169], v[160:163], v[144:159]\n\t" \
    "ds_read_b64_tr_b16 v[166:167], v200 offset:20480\n\t" \
    "v_exp_f32 v64, v64\n\t" \
    "ds_read_b64_tr_b16 v[168:169], v200 offset:20544\n\t" \
    "v_exp_f32 v65, v65\n\t" \
    "v_mfma_f32_32x32x16_bf16 v[128:143], v[188:191], v[196:199], v[128:143]\n\t" \
    "s_mov_b64 exec, %[vmask]\n\t" \
    "ds_read_b128 v[176:179], v202 offset:10240\n\t" \
    "ds_read_b128 v[180:183], v202 offset:10880\n\t" \
    "ds_read_b128 v[184:187], v202 offset:11520\n\t" \
    "ds_read_b128 v[188:191], v202 offset:12160\n\t" \
    "s_mov_b64 exec, -1\n\t" \
    "v_exp_f32 v66, v66\n\t" \
    "v_exp_f32 v67, v67\n\t" \
    "v_exp_f32 v68, v68\n\t" \
    "v_mfma_f32_32x32x16_bf16 v[112:127], v[170:173], v[160:163], v[144:159]\n\t" \
    "ds_read_b64_tr_b16 v[170:171], v200 offset:21760\n\t" \
    "v_exp_f32 v69, v69\n\t" \
    "ds_read_b64_tr_b16 v[172:173], v200 offset:21824\n\t" \
    "v_exp_f32 v70, v70\n\t" \
    "s_add_u32 m0, m0, 0x2000\n\t" \
    "v_add_u32 v203, 0x2000, v203\n\t" \
    "global_load_lds_dwordx4 v203, %[kv] sc1\n\t" \
    "v_exp_f32 v71, v71\n\t" \
    "v_cvt_pk_bf16_f32 v192, v64, v65\n\t" \
    "v_cvt_pk_bf16_f32 v193, v66, v67\n\t" \
    "v_cvt_pk_bf16_f32 v194, v68, v69\n\t" \
    "v_cvt_pk_bf16_f32 v195, v70, v71\n\t" \
    "v_exp_f32 v72, v72\n\t" \
    "v_exp_f32 v73, v73\n\t" \
    "v_exp_f32 v74, v74\n\t" \
    "v_exp_f32 v75, v75\n\t" \
    "v_mfma_f32_32x32x4_2b_bf16 v[96:127], v[174:175], v[164:165], v[96:127]\n\t" \
    "ds_read_b64_tr_b16 v[174:175], v201 offset:20480\n\t" \
    "v_exp_f32 v76, v76\n\t" \
    "v_exp_f32 v77, v77\n\t" \
    "s_add_u32 m0, m0, 0x2000\n\t" \
    "v_add_u32 v203, 0x2000, v203\n\t" \
    "global_load_lds_dwordx4 v203, %[kv] sc1\n\t" \
    "v_exp_f32 v78, v78\n\t" \
    "v_exp_f32 v79, v79\n\t" \
    "v_cvt_pk_bf16_f32 v196, v72, v73\n\t" \
    "v_cvt_pk_bf16_f32 v197, v74, v75\n\t" \
    "v_cvt_pk_bf16_f32 v198, v76, v77\n\t" \
    "v_cvt_pk_bf16_f32 v199, v78, v79\n\t" \
    "v_exp_f32 v80, v80\n\t" \
    "s_setprio 2\n\t" \
    "s_waitcnt lgkmcnt(3)\n\t" \
    "v_exp_f32 v81, v81\n\t" \
    "v_mfma_f32_32x32x16_bf16 v[128:143], v[176:179], v[192:195], v[128:143]\n\t" \
    "v_exp_f32 v82, v82\n\t" \
    "v_exp_f32 v83, v83\n\t" \
    "s_add_u32 m0, m0, 0x2000\n\t" \
    "v_add_u32 v203, 0x2000, v203\n\t" \
    "global_load_lds_dwordx4 v203, %[kv] sc1\n\t" \
    "v_exp_f32 v84, v84\n\t" \
    "v_exp_f32 v85, v85\n\t" \
    "v_mfma_f32_32x32x16_bf16 v[128:143], v[180:183], v[196:199], v[128:143]\n\t" \
    "v_exp_f32 v86, v86\n\t" \
    "v_exp_f32 v87, v87\n\t" \
    "v_cvt_pk_bf16_f32 v192, v80, v81\n\t" \
    "v_cvt_pk_bf16_f32 v193, v82, v83\n\t" \
    "v_cvt_pk_bf16_f32 v194, v84, v85\n\t" \
    "v_cvt_pk_bf16_f32 v195, v86, v87\n\t" \
    "v_exp_f32 v88, v88\n\t" \
    "v_exp_f32 v89, v89\n\t" \
    "v_mfma_f32_32x32x16_bf16 v[128:143], v[184:187], v[192:195], v[128:143]\n\t" \
    "v_exp_f32 v90, v90\n\t" \
    "v_exp_f32 v91, v91\n\t" \
    "s_add_u32 m0, m0, 0x2000\n\t" \
    "v_add_u32 v203, 0x2000, v203\n\t" \
    "global_load_lds_dwordx4 v203, %[kv] sc1\n\t" \
    "v_exp_f32 v92, v92\n\t" \
    "v_exp_f32 v93, v93\n\t" \
    "s_add_u32 m0, m0, 0x2000\n\t" \
    "v_add_u32 v203, 0x2000, v203\n\t" \
    "global_load_lds_dwordx4 v203, %[kv] sc1\n\t" \
    "v_exp_f32 v94, v94\n\t" \
    "v_exp_f32 v95, v95\n\t" \
    "v_cvt_pk_bf16_f32 v196, v88, v89\n\t" \
    "v_cvt_pk_bf16_f32 v197, v90, v91\n\t" \
    "v_cvt_pk_bf16_f32 v198, v92, v93\n\t" \
    "v_cvt_pk_bf16_f32 v199, v94, v95\n\t" \
    "s_setprio 1\n\t" \
    "s_waitcnt lgkmcnt(0)\n\t" \
    "v_mfma_f32_32x32x16_bf16 v[64:79], v[166:169], v[160:163], v[144:159]\n\t" \
    "ds_read_b64_tr_b16 v[166:167], v200 offset:25600\n\t" \
    "v_exp_f32 v96, v96\n\t" \
    "ds_read_b64_tr_b16 v[168:169], v200 offset:25664\n\t" \
    "v_exp_f32 v97, v97\n\t" \
    "v_mfma_f32_32x32x16_bf16 v[128:143], v[188:191], v[196:199], v[128:143]\n\t" \
    "s_mov_b64 exec, %[vmask]\n\t" \
    "ds_read_b128 v[176:179], v202 offset:15360\n\t" \
    "ds_read_b128 v[180:183], v202 offset:16000\n\t" \
    "ds_read_b128 v[184:187], v202 offset:16640\n\t" \
    "ds_read_b128 v[188:191], v202 offset:17280\n\t" \
    "s_mov_b64 exec, -1\n\t" \
    "v_exp_f32 v98, v98\n\t" \
    "v_exp_f32 v99, v99\n\t" \
    "v_exp_f32 v100, v100\n\t" \
    "v_mfma_f32_32x32x16_bf16 v[80:95], v[170:173], v[160:163], v[144:159]\n\t" \
    "ds_read_b64_tr_b16 v[170:171], v200 offset:26880\n\t" \
    "v_exp_f32 v101, v101\n\t" \
    "ds_read_b64_tr_b16 v[172:173], v200 offset:26944\n\t" \
    "v_exp_f32 v102, v102\n\t" \
    "v_exp_f32 v103, v103\n\t" \
    "v_cvt_pk_bf16_f32 v192, v96, v97\n\t" \
    "v_cvt_pk_bf16_f32 v193, v98, v99\n\t" \
    "v_cvt_pk_bf16_f32 v194, v100, v101\n\t" \
    "v_cvt_pk_bf16_f32 v195, v102, v103\n\t" \
    "v_exp_f32 v104, v104\n\t" \
    "v_exp_f32 v105, v105\n\t" \
    "v_exp_f32 v106, v106\n\t" \
    "v_exp_f32 v107, v107\n\t" \
    "v_mfma_f32_32x32x4_2b_bf16 v[64:95], v[174:175], v[164:165], v[64:95]\n\t" \
    "ds_read_b64_tr_b16 v[174:175], v201 offset:25600\n\t" \
    "v_exp_f32 v108, v108\n\t" \
    "v_exp_f32 v109, v109\n\t" \
    "v_exp_f32 v110, v110\n\t" \
    "v_exp_f32 v111, v111\n\t" \
    "v_cvt_pk_bf16_f32 v196, v104, v105\n\t" \
    "v_cvt_pk_bf16_f32 v197, v106, v107\n\t" \
    "v_cvt_pk_bf16_f32 v198, v108, v109\n\t" \
    "v_cvt_pk_bf16_f32 v199, v110, v111\n\t" \
    "v_exp_f32 v112, v112\n\t" \
    "s_setprio 0\n\t" \
    "s_waitcnt lgkmcnt(3)\n\t" \
    "v_exp_f32 v113, v113\n\t" \
    "v_mfma_f32_32x32x16_bf16 v[128:143], v[176:179], v[192:195], v[128:143]\n\t" \
    "v_exp_f32 v114, v114\n\t" \
    "v_exp_f32 v115, v115\n\t" \
    "v_exp_f32 v116, v116\n\t" \
    "v_exp_f32 v117, v117\n\t" \
    "v_mfma_f32_32x32x16_bf16 v[128:143], v[180:183], v[196:199], v[128:143]\n\t" \
    "v_exp_f32 v118, v118\n\t" \
    "v_exp_f32 v119, v119\n\t" \
    "v_cvt_pk_bf16_f32 v192, v112, v113\n\t" \
    "v_cvt_pk_bf16_f32 v193, v114, v115\n\t" \
    "v_cvt_pk_bf16_f32 v194, v116, v117\n\t" \
    "v_cvt_pk_bf16_f32 v195, v118, v119\n\t" \
    "v_exp_f32 v120, v120\n\t" \
    "v_exp_f32 v121, v121\n\t" \
    "v_mfma_f32_32x32x16_bf16 v[128:143], v[184:187], v[192:195], v[128:143]\n\t" \
    "v_exp_f32 v122, v122\n\t" \
    "v_exp_f32 v123, v123\n\t" \
    "v_exp_f32 v124, v124\n\t" \
    "v_exp_f32 v125, v125\n\t" \
    "v_exp_f32 v126, v126\n\t" \
    "v_exp_f32 v127, v127\n\t" \
    "v_cvt_pk_bf16_f32 v196, v120, v121\n\t" \
    "v_cvt_pk_bf16_f32 v197, v122, v123\n\t" \
    "v_cvt_pk_bf16_f32 v198, v124, v125\n\t" \
    "v_cvt_pk_bf16_f32 v199, v126, v127\n\t" \
    "v_add_u32 v200, 20480, v200\n\t" \
    "v_add_u32 v201, 20480, v201\n\t" \
    "v_add_u32 v202, 20480, v202\n\t" \
    "s_waitcnt vmcnt(0)\n\t" \
    "s_barrier\n\t" \
    "s_setprio 3\n\t" \
    "s_waitcnt lgkmcnt(0)\n\t" \
    "v_mfma_f32_32x32x16_bf16 v[96:111], v[166:169], v[160:163], v[144:159]\n\t" \
    "ds_read_b64_tr_b16 v[166:167], v200 offset:10240\n\t" \
    "v_exp_f32 v64, v64\n\t" \
    "ds_read_b64_tr_b16 v[168:169], v200 offset:10304\n\t" \
    "v_exp_f32 v65, v65\n\t" \
    "v_mfma_f32_32x32x16_bf16 v[128:143], v[188:191], v[196:199], v[128:143]\n\t" \
    "s_mov_b64 exec, %[vmask]\n\t" \
    "ds_read_b128 v[176:179], v202 offset:0\n\t" \
    "ds_read_b128 v[180:183], v202 offset:640\n\t" \
    "ds_read_b128 v[184:187], v202 offset:1280\n\t" \
    "ds_read_b128 v[188:191], v202 offset:1920\n\t" \
    "s_mov_b64 exec, -1\n\t" \
    "v_exp_f32 v66, v66\n\t" \
    "v_exp_f32 v67, v67\n\t" \
    "v_mov_b32 v224, -1\n\t" \
    "v_mov_b32 v225, -1\n\t" \
    "v_mov_b32 v226, -1\n\t" \
    "v_mov_b32 v227, -1\n\t" \
    "buffer_store_dwordx4 v[224:227], %[po0], %[wr], 0 offen offset:0 sc1\n\t" \
    "v_exp_f32 v68, v68\n\t" \
    "v_mfma_f32_32x32x16_bf16 v[112:127], v[170:173], v[160:163], v[144:159]\n\t" \
    "ds_read_b64_tr_b16 v[170:171], v200 offset:11520\n\t" \
    "v_exp_f32 v69, v69\n\t" \
    "ds_read_b64_tr_b16 v[172:173], v200 offset:11584\n\t" \
    "v_exp_f32 v70, v70\n\t" \
    "v_exp_f32 v71, v71\n\t" \
    "v_cvt_pk_bf16_f32 v192, v64, v65\n\t" \
    "v_cvt_pk_bf16_f32 v193, v66, v67\n\t" \
    "v_cvt_pk_bf16_f32 v194, v68, v69\n\t" \
    "v_cvt_pk_bf16_f32 v195, v70, v71\n\t" \
    "v_exp_f32 v72, v72\n\t" \
    "v_exp_f32 v73, v73\n\t" \
    "v_exp_f32 v74, v74\n\t" \
    "v_exp_f32 v75, v75\n\t" \
    "v_mfma_f32_32x32x4_2b_bf16 v[96:127], v[174:175], v[164:165], v[96:127]\n\t" \
    "ds_read_b64_tr_b16 v[174:175], v201 offset:10240\n\t" \
    "v_exp_f32 v76, v76\n\t" \
    "v_exp_f32 v77, v77\n\t" \
    "buffer_store_dwordx4 v[224:227], %[po0], %[wr], 0 offen offset:1024 sc1\n\t" \
    "v_exp_f32 v78, v78\n\t" \
    "v_exp_f32 v79, v79\n\t" \
    "v_cvt_pk_bf16_f32 v196, v72, v73\n\t" \
    "v_cvt_pk_bf16_f32 v197, v74, v75\n\t" \
    "v_cvt_pk_bf16_f32 v198, v76, v77\n\t" \
    "v_cvt_pk_bf16_f32 v199, v78, v79\n\t" \
    "v_exp_f32 v80, v80\n\t" \
    "s_setprio 2\n\t" \
    "s_waitcnt lgkmcnt(3)\n\t" \
    "v_exp_f32 v81, v81\n\t" \
    "v_mfma_f32_32x32x16_bf16 v[128:143], v[176:179], v[192:195], v[128:143]\n\t" \
    "v_exp_f32 v82, v82\n\t" \
    "v_exp_f32 v83, v83\n\t" \
    "v_exp_f32 v84, v84\n\t" \
    "v_exp_f32 v85, v85\n\t" \
    "v_mfma_f32_32x32x16_bf16 v[128:143], v[180:183], v[196:199], v[128:143]\n\t" \
    "v_exp_f32 v86, v86\n\t" \
    "v_exp_f32 v87, v87\n\t" \
    "buffer_store_dwordx4 v[224:227], %[po0], %[wr], 0 offen offset:2048 sc1\n\t" \
    "v_cvt_pk_bf16_f32 v192, v80, v81\n\t" \
    "v_cvt_pk_bf16_f32 v193, v82, v83\n\t" \
    "v_cvt_pk_bf16_f32 v194, v84, v85\n\t" \
    "v_cvt_pk_bf16_f32 v195, v86, v87\n\t" \
    "v_exp_f32 v88, v88\n\t" \
    "v_exp_f32 v89, v89\n\t" \
    "v_mfma_f32_32x32x16_bf16 v[128:143], v[184:187], v[192:195], v[128:143]\n\t" \
    "v_exp_f32 v90, v90\n\t" \
    "v_exp_f32 v91, v91\n\t" \
    "v_exp_f32 v92, v92\n\t" \
    "v_exp_f32 v93, v93\n\t" \
    "v_exp_f32 v94, v94\n\t" \
    "v_exp_f32 v95, v95\n\t" \
    "v_cvt_pk_bf16_f32 v196, v88, v89\n\t" \
    "v_cvt_pk_bf16_f32 v197, v90, v91\n\t" \
    "v_cvt_pk_bf16_f32 v198, v92, v93\n\t" \
    "v_cvt_pk_bf16_f32 v199, v94, v95\n\t" \
    "s_setprio 1\n\t" \
    "s_waitcnt lgkmcnt(0)\n\t" \
    "v_mfma_f32_32x32x16_bf16 v[64:79], v[166:169], v[160:163], v[144:159]\n\t" \
    "ds_read_b64_tr_b16 v[166:167], v200 offset:15360\n\t" \
    "v_exp_f32 v96, v96\n\t" \
    "ds_read_b64_tr_b16 v[168:169], v200 offset:15424\n\t" \
    "v_exp_f32 v97, v97\n\t" \
    "buffer_store_dwordx4 v[224:227], %[po0], %[wr], 0 offen offset:3072 sc1\n\t" \
    "v_mfma_f32_32x32x16_bf16 v[128:143], v[188:191], v[196:199], v[128:143]\n\t" \
    "s_mov_b64 exec, %[vmask]\n\t" \
    "ds_read_b128 v[176:179], v202 offset:5120\n\t" \
    "ds_read_b128 v[180:183], v202 offset:5760\n\t" \
    "ds_read_b128 v[184:187], v202 offset:6400\n\t" \
    "ds_read_b128 v[188:191], v202 offset:7040\n\t" \
    "s_mov_b64 exec, -1\n\t" \
    "v_exp_f32 v98, v98\n\t" \
    "v_exp_f32 v99, v99\n\t" \
    "v_exp_f32 v100, v100\n\t" \
    "v_mfma_f32_32x32x16_bf16 v[80:95], v[170:173], v[160:163], v[144:159]\n\t" \
    "ds_read_b64_tr_b16 v[170:171], v200 offset:16640\n\t" \
    "v_exp_f32 v101, v101\n\t" \
    "ds_read_b64_tr_b16 v[172:173], v200 offset:16704\n\t" \
    "v_exp_f32 v102, v102\n\t" \
    "v_exp_f32 v103, v103\n\t" \
    "v_cvt_pk_bf16_f32 v192, v96, v97\n\t" \
    "v_cvt_pk_bf16_f32 v193, v98, v99\n\t" \
    "v_cvt_pk_bf16_f32 v194, v100, v101\n\t" \
    "v_cvt_pk_bf16_f32 v195, v102, v103\n\t" \
    "v_exp_f32 v104, v104\n\t" \
    "v_exp_f32 v105, v105\n\t" \
    "v_exp_f32 v106, v106\n\t" \
    "v_exp_f32 v107, v107\n\t" \
    "buffer_store_dwordx4 v[224:227], %[po1], %[wr], 0 offen sc1\n\t" \
    "v_mfma_f32_32x32x4_2b_bf16 v[64:95], v[174:175], v[164:165], v[64:95]\n\t" \
    "ds_read_b64_tr_b16 v[174:175], v201 offset:15360\n\t" \
    "v_exp_f32 v108, v108\n\t" \
    "v_exp_f32 v109, v109\n\t" \
    "v_exp_f32 v110, v110\n\t" \
    "v_exp_f32 v111, v111\n\t" \
    "v_cvt_pk_bf16_f32 v196, v104, v105\n\t" \
    "v_cvt_pk_bf16_f32 v197, v106, v107\n\t" \
    "v_cvt_pk_bf16_f32 v198, v108, v109\n\t" \
    "v_cvt_pk_bf16_f32 v199, v110, v111\n\t" \
    "v_exp_f32 v112, v112\n\t" \
    "s_setprio 0\n\t" \
    "s_waitcnt lgkmcnt(3)\n\t" \
    "v_exp_f32 v113, v113\n\t" \
    "v_mfma_f32_32x32x16_bf16 v[128:143], v[176:179], v[192:195], v[128:143]\n\t" \
    "v_exp_f32 v114, v114\n\t" \
    "v_exp_f32 v115, v115\n\t" \
    "v_exp_f32 v116, v116\n\t" \
    "v_exp_f32 v117, v117\n\t" \
    "buffer_store_dwordx4 v[224:227], %[po2], %[wr], 0 offen sc1\n\t" \
    "v_mfma_f32_32x32x16_bf16 v[128:143], v[180:183], v[196:199], v[128:143]\n\t" \
    "v_exp_f32 v118, v118\n\t" \
    "v_exp_f32 v119, v119\n\t" \
    "v_cvt_pk_bf16_f32 v192, v112, v113\n\t" \
    "v_cvt_pk_bf16_f32 v193, v114, v115\n\t" \
    "v_cvt_pk_bf16_f32 v194, v116, v117\n\t" \
    "v_cvt_pk_bf16_f32 v195, v118, v119\n\t" \
    "v_exp_f32 v120, v120\n\t" \
    "v_exp_f32 v121, v121\n\t" \
    "v_mfma_f32_32x32x16_bf16 v[128:143], v[184:187], v[192:195], v[128:143]\n\t" \
    "v_exp_f32 v122, v122\n\t" \
    "v_exp_f32 v123, v123\n\t" \
    "v_exp_f32 v124, v124\n\t" \
    "v_exp_f32 v125, v125\n\t" \
    "v_exp_f32 v126, v126\n\t" \
    "v_exp_f32 v127, v127\n\t" \
    "v_cvt_pk_bf16_f32 v196, v120, v121\n\t" \
    "v_cvt_pk_bf16_f32 v197, v122, v123\n\t" \
    "v_cvt_pk_bf16_f32 v198, v124, v125\n\t" \
    "v_cvt_pk_bf16_f32 v199, v126, v127\n\t" \
    "s_setprio 3\n\t" \
    "s_waitcnt lgkmcnt(0)\n\t" \
    "v_mfma_f32_32x32x16_bf16 v[96:111], v[166:169], v[160:163], v[144:159]\n\t" \
    "ds_read_b64_tr_b16 v[166:167], v200 offset:20480\n\t" \
    "v_exp_f32 v64, v64\n\t" \
    "ds_read_b64_tr_b16 v[168:169], v200 offset:20544\n\t" \
    "v_exp_f32 v65, v65\n\t" \
    "v_mfma_f32_32x32x16_bf16 v[128:143], v[188:191], v[196:199], v[128:143]\n\t" \
    "s_mov_b64 exec, %[vmask]\n\t" \
    "ds_read_b128 v[176:179], v202 offset:10240\n\t" \
    "ds_read_b128 v[180:183], v202 offset:10880\n\t" \
    "ds_read_b128 v[184:187], v202 offset:11520\n\t" \
    "ds_read_b128 v[188:191], v202 offset:12160\n\t" \
    "s_mov_b64 exec, -1\n\t" \
    "v_exp_f32 v66, v66\n\t" \
    "v_exp_f32 v67, v67\n\t" \
    "global_load_dword v204, %[rs0], %[rw0] offset:0\n\t" \
    "v_exp_f32 v68, v68\n\t" \
    "v_mfma_f32_32x32x16_bf16 v[112:127], v[170:173], v[160:163], v[144:159]\n\t" \
    "ds_read_b64_tr_b16 v[170:171], v200 offset:21760\n\t" \
    "v_exp_f32 v69, v69\n\t" \
    "ds_read_b64_tr_b16 v[172:173], v200 offset:21824\n\t" \
    "v_exp_f32 v70, v70\n\t" \
    "v_exp_f32 v71, v71\n\t" \
    "global_load_dword v205, %[rs0], %[rw0] offset:3072\n\t" \
    "v_cvt_pk_bf16_f32 v192, v64, v65\n\t" \
    "v_cvt_pk_bf16_f32 v193, v66, v67\n\t" \
    "v_cvt_pk_bf16_f32 v194, v68, v69\n\t" \
    "v_cvt_pk_bf16_f32 v195, v70, v71\n\t" \
    "v_exp_f32 v72, v72\n\t" \
    "v_exp_f32 v73, v73\n\t" \
    "v_exp_f32 v74, v74\n\t" \
    "v_exp_f32 v75, v75\n\t" \
    "v_add_u32 v228, 6144, %[rs0]\n\t" \
    "global_load_dword v206, v228, %[rw0] offset:0\n\t" \
    "v_mfma_f32_32x32x4_2b_bf16 v[96:127], v[174:175], v[164:165], v[96:127]\n\t" \
    "ds_read_b64_tr_b16 v[174:175], v201 offset:20480\n\t" \
    "v_exp_f32 v76, v76\n\t" \
    "v_exp_f32 v77, v77\n\t" \
    "v_exp_f32 v78, v78\n\t" \
    "v_exp_f32 v79, v79\n\t" \
    "global_load_dword v207, v228, %[rw0] offset:3072\n\t" \
    "v_cvt_pk_bf16_f32 v196, v72, v73\n\t" \
    "v_cvt_pk_bf16_f32 v197, v74, v75\n\t" \
    "v_cvt_pk_bf16_f32 v198, v76, v77\n\t" \
    "v_cvt_pk_bf16_f32 v199, v78, v79\n\t" \
    "v_exp_f32 v80, v80\n\t" \
    "s_setprio 2\n\t" \
    "s_waitcnt lgkmcnt(3)\n\t" \
    "v_exp_f32 v81, v81\n\t" \
    "v_mfma_f32_32x32x16_bf16 v[128:143], v[176:179], v[192:195], v[128:143]\n\t" \
    "v_exp_f32 v82, v82\n\t" \
    "v_exp_f32 v83, v83\n\t" \
    "global_load_dword v208, %[rt0], %[rw0] offset:0\n\t" \
    "v_exp_f32 v84, v84\n\t" \
    "v_exp_f32 v85, v85\n\t" \
    "v_mfma_f32_32x32x16_bf16 v[128:143], v[180:183], v[196:199], v[128:143]\n\t" \
    "v_exp_f32 v86, v86\n\t" \
    "v_exp_f32 v87, v87\n\t" \
    "global_load_dword v209, %[rt0], %[rw0] offset:3072\n\t" \
    "v_cvt_pk_bf16_f32 v192, v80, v81\n\t" \
    "v_cvt_pk_bf16_f32 v193, v82, v83\n\t" \
    "v_cvt_pk_bf16_f32 v194, v84, v85\n\t" \
    "v_cvt_pk_bf16_f32 v195, v86, v87\n\t" \
    "v_exp_f32 v88, v88\n\t" \
    "v_exp_f32 v89, v89\n\t" \
    "v_mfma_f32_32x32x16_bf16 v[128:143], v[184:187], v[192:195], v[128:143]\n\t" \
    "v_exp_f32 v90, v90\n\t" \
    "v_exp_f32 v91, v91\n\t" \
    "v_add_u32 v228, 6144, %[rt0]\n\t" \
    "global_load_dword v210, v228, %[rw0] offset:0\n\t" \
    "v_exp_f32 v92, v92\n\t" \
    "v_exp_f32 v93, v93\n\t" \
    "v_exp_f32 v94, v94\n\t" \
    "v_exp_f32 v95, v95\n\t" \
    "global_load_dword v211, v228, %[rw0] offset:3072\n\t" \
    "v_cvt_pk_bf16_f32 v196, v88, v89\n\t" \
    "v_cvt_pk_bf16_f32 v197, v90, v91\n\t" \
    "v_cvt_pk_bf16_f32 v198, v92, v93\n\t" \
    "v_cvt_pk_bf16_f32 v199, v94, v95\n\t" \
    "s_setprio 1\n\t" \
    "s_waitcnt lgkmcnt(0)\n\t" \
    "v_mfma_f32_32x32x16_bf16 v[64:79], v[166:169], v[160:163], v[144:159]\n\t" \
    "ds_read_b64_tr_b16 v[166:167], v200 offset:25600\n\t" \
    "v_exp_f32 v96, v96\n\t" \
    "ds_read_b64_tr_b16 v[168:169], v200 offset:25664\n\t" \
    "v_exp_f32 v97, v97\n\t" \
    "v_mfma_f32_32x32x16_bf16 v[128:143], v[188:191], v[196:199], v[128:143]\n\t" \
    "s_mov_b64 exec, %[vmask]\n\t" \
    "ds_read_b128 v[176:179], v202 offset:15360\n\t" \
    "ds_read_b128 v[180:183], v202 offset:16000\n\t" \
    "ds_read_b128 v[184:187], v202 offset:16640\n\t" \
    "ds_read_b128 v[188:191], v202 offset:17280\n\t" \
    "s_mov_b64 exec, -1\n\t" \
    "v_exp_f32 v98, v98\n\t" \
    "v_exp_f32 v99, v99\n\t" \
    "global_load_dword v212, %[rs1], %[rw1] offset:0\n\t" \
    "v_exp_f32 v100, v100\n\t" \
    "v_mfma_f32_32x32x16_bf16 v[80:95], v[170:173], v[160:163], v[144:159]\n\t" \
    "ds_read_b64_tr_b16 v[170:171], v200 offset:26880\n\t" \
    "v_exp_f32 v101, v101\n\t" \
    "ds_read_b64_tr_b16 v[172:173], v200 offset:26944\n\t" \
    "v_exp_f32 v102, v102\n\t" \
    "v_exp_f32 v103, v103\n\t" \
    "global_load_dword v213, %[rs1], %[rw1] offset:1280\n\t" \
    "v_cvt_pk_bf16_f32 v192, v96, v97\n\t" \
    "v_cvt_pk_bf16_f32 v193, v98, v99\n\t" \
    "v_cvt_pk_bf16_f32 v194, v100, v101\n\t" \
    "v_cvt_pk_bf16_f32 v195, v102, v103\n\t" \
    "v_exp_f32 v104, v104\n\t" \
    "v_exp_f32 v105, v105\n\t" \
    "v_exp_f32 v106, v106\n\t" \
    "v_exp_f32 v107, v107\n\t" \
    "global_load_dword v214, %[rs1], %[rw1] offset:2560\n\t" \
    "v_mfma_f32_32x32x4_2b_bf16 v[64:95], v[174:175], v[164:165], v[64:95]\n\t" \
    "ds_read_b64_tr_b16 v[174:175], v201 offset:25600\n\t" \
    "v_exp_f32 v108, v108\n\t" \
    "v_exp_f32 v109, v109\n\t" \
    "v_exp_f32 v110, v110\n\t" \
    "v_exp_f32 v111, v111\n\t" \
    "global_load_dword v215, %[rs1], %[rw1] offset:3840\n\t" \
    "v_cvt_pk_bf16_f32 v196, v104, v105\n\t" \
    "v_cvt_pk_bf16_f32 v197, v106, v107\n\t" \
    "v_cvt_pk_bf16_f32 v198, v108, v109\n\t" \
    "v_cvt_pk_bf16_f32 v199, v110, v111\n\t" \
    "v_exp_f32 v112, v112\n\t" \
    "s_setprio 0\n\t" \
    "s_waitcnt lgkmcnt(3)\n\t" \
    "v_exp_f32 v113, v113\n\t" \
    "v_mfma_f32_32x32x16_bf16 v[128:143], v[176:179], v[192:195], v[128:143]\n\t" \
    "v_exp_f32 v114, v114\n\t" \
    "v_exp_f32 v115, v115\n\t" \
    "global_load_dword v216, %[rt1], %[rw1] offset:0\n\t" \
    "v_exp_f32 v116, v116\n\t" \
    "v_exp_f32 v117, v117\n\t" \
    "v_mfma_f32_32x32x16_bf16 v[128:143], v[180:183], v[196:199], v[128:143]\n\t" \
    "v_exp_f32 v118, v118\n\t" \
    "v_exp_f32 v119, v119\n\t" \
    "global_load_dword v217, %[rt1], %[rw1] offset:1280\n\t" \
    "v_cvt_pk_bf16_f32 v192, v112, v113\n\t" \
    "v_cvt_pk_bf16_f32 v193, v114, v115\n\t" \
    "v_cvt_pk_bf16_f32 v194, v116, v117\n\t" \
    "v_cvt_pk_bf16_f32 v195, v118, v119\n\t" \
    "v_exp_f32 v120, v120\n\t" \
    "v_exp_f32 v121, v121\n\t" \
    "v_mfma_f32_32x32x16_bf16 v[128:143], v[184:187], v[192:195], v[128:143]\n\t" \
    "v_exp_f32 v122, v122\n\t" \
    "v_exp_f32 v123, v123\n\t" \
    "global_load_dword v218, %[rt1], %[rw1] offset:2560\n\t" \
    "v_exp_f32 v124, v124\n\t" \
    "v_exp_f32 v125, v125\n\t" \
    "v_exp_f32 v126, v126\n\t" \
    "v_exp_f32 v127, v127\n\t" \
    "global_load_dword v219, %[rt1], %[rw1] offset:3840\n\t" \
    "v_cvt_pk_bf16_f32 v196, v120, v121\n\t" \
    "v_cvt_pk_bf16_f32 v197, v122, v123\n\t" \
    "v_cvt_pk_bf16_f32 v198, v124, v125\n\t" \
    "v_cvt_pk_bf16_f32 v199, v126, v127\n\t" \
    "v_add_u32 v200, 20480, v200\n\t" \
    "v_add_u32 v201, 20480, v201\n\t" \
    "v_add_u32 v202, 20480, v202\n\t" \
    "s_mov_b32 %[cnt], 5\n\t" \
    ".p2align 6\n\t" \
    "LB%=:\n\t" \
    "s_cmp_eq_u32 %[cnt], 3\n\t" \
    "s_cbranch_scc1 LX%=\n\t" \
    "s_setprio 3\n\t" \
    "s_waitcnt lgkmcnt(0)\n\t" \
    "v_mfma_f32_32x32x16_bf16 v[96:111], v[166:169], v[160:163], v[144:159]\n\t" \
    "ds_read_b64_tr_b16 v[166:167], v200 offset:10240\n\t" \
    "v_exp_f32 v64, v64\n\t" \
    "ds_read_b64_tr_b16 v[168:169], v200 offset:10304\n\t" \
    "v_exp_f32 v65, v65\n\t" \
    "v_mfma_f32_32x32x16_bf16 v[128:143], v[188:191], v[196:199], v[128:143]\n\t" \
    "s_mov_b64 exec, %[vmask]\n\t" \
    "ds_read_b128 v[176:179], v202 offset:0\n\t" \
    "ds_read_b128 v[180:183], v202 offset:640\n\t" \
    "ds_read_b128 v[184:187], v202 offset:1280\n\t" \
    "ds_read_b128 v[188:191], v202 offset:1920\n\t" \
    "s_mov_b64 exec, -1\n\t" \
    "v_exp_f32 v66, v66\n\t" \
    "v_exp_f32 v67, v67\n\t" \
    "v_exp_f32 v68, v68\n\t" \
    "v_mfma_f32_32x32x16_bf16 v[112:127], v[170:173], v[160:163], v[144:159]\n\t" \
    "ds_read_b64_tr_b16 v[170:171], v200 offset:11520\n\t" \
    "v_exp_f32 v69, v69\n\t" \
    "ds_read_b64_tr_b16 v[172:173], v200 offset:11584\n\t" \
    "v_exp_f32 v70, v70\n\t" \
    "v_exp_f32 v71, v71\n\t" \
    "v_cvt_pk_bf16_f32 v192, v64, v65\n\t" \
    "v_cvt_pk_bf16_f32 v193, v66, v67\n\t" \
    "v_cvt_pk_bf16_f32 v194, v68, v69\n\t" \
    "v_cvt_pk_bf16_f32 v195, v70, v71\n\t" \
    "v_exp_f32 v72, v72\n\t" \
    "v_exp_f32 v73, v73\n\t" \
    "v_exp_f32 v74, v74\n\t" \
    "v_exp_f32 v75, v75\n\t" \
    "v_mfma_f32_32x32x4_2b_bf16 v[96:127], v[174:175], v[164:165], v[96:127]\n\t" \
    "ds_read_b64_tr_b16 v[174:175], v201 offset:10240\n\t" \
    "v_exp_f32 v76, v76\n\t" \
    "v_exp_f32 v77, v77\n\t" \
    "v_exp_f32 v78, v78\n\t" \
    "v_exp_f32 v79, v79\n\t" \
    "v_cvt_pk_bf16_f32 v196, v72, v73\n\t" \
    "v_cvt_pk_bf16_f32 v197, v74, v75\n\t" \
    "v_cvt_pk_bf16_f32 v198, v76, v77\n\t" \
    "v_cvt_pk_bf16_f32 v199, v78, v79\n\t" \
    "v_exp_f32 v80, v80\n\t" \
    "s_setprio 2\n\t" \
    "s_waitcnt lgkmcnt(3)\n\t" \
    "v_exp_f32 v81, v81\n\t" \
    "v_mfma_f32_32x32x16_bf16 v[128:143], v[176:179], v[192:195], v[128:143]\n\t" \
    "v_exp_f32 v82, v82\n\t" \
    "v_exp_f32 v83, v83\n\t" \
    "v_exp_f32 v84, v84\n\t" \
    "v_exp_f32 v85, v85\n\t" \
    "v_mfma_f32_32x32x16_bf16 v[128:143], v[180:183], v[196:199], v[128:143]\n\t" \
    "v_exp_f32 v86, v86\n\t" \
    "v_exp_f32 v87, v87\n\t" \
    "v_cvt_pk_bf16_f32 v192, v80, v81\n\t" \
    "v_cvt_pk_bf16_f32 v193, v82, v83\n\t" \
    "v_cvt_pk_bf16_f32 v194, v84, v85\n\t" \
    "v_cvt_pk_bf16_f32 v195, v86, v87\n\t" \
    "v_exp_f32 v88, v88\n\t" \
    "v_exp_f32 v89, v89\n\t" \
    "v_mfma_f32_32x32x16_bf16 v[128:143], v[184:187], v[192:195], v[128:143]\n\t" \
    "v_exp_f32 v90, v90\n\t" \
    "v_exp_f32 v91, v91\n\t" \
    "v_exp_f32 v92, v92\n\t" \
    "v_exp_f32 v93, v93\n\t" \
    "v_exp_f32 v94, v94\n\t" \
    "v_exp_f32 v95, v95\n\t" \
    "v_cvt_pk_bf16_f32 v196, v88, v89\n\t" \
    "v_cvt_pk_bf16_f32 v197, v90, v91\n\t" \
    "v_cvt_pk_bf16_f32 v198, v92, v93\n\t" \
    "v_cvt_pk_bf16_f32 v199, v94, v95\n\t" \
    "s_setprio 1\n\t" \
    "s_waitcnt lgkmcnt(0)\n\t" \
    "v_mfma_f32_32x32x16_bf16 v[64:79], v[166:169], v[160:163], v[144:159]\n\t" \
    "ds_read_b64_tr_b16 v[166:167], v200 offset:15360\n\t" \
    "v_exp_f32 v96, v96\n\t" \
    "ds_read_b64_tr_b16 v[168:169], v200 offset:15424\n\t" \
    "v_exp_f32 v97, v97\n\t" \
    "v_mfma_f32_32x32x16_bf16 v[128:143], v[188:191], v[196:199], v[128:143]\n\t" \
    "s_mov_b64 exec, %[vmask]\n\t" \
    "ds_read_b128 v[176:179], v202 offset:5120\n\t" \
    "ds_read_b128 v[180:183], v202 offset:5760\n\t" \
    "ds_read_b128 v[184:187], v202 offset:6400\n\t" \
    "ds_read_b128 v[188:191], v202 offset:7040\n\t" \
    "s_mov_b64 exec, -1\n\t" \
    "v_exp_f32 v98, v98\n\t" \
    "v_exp_f32 v99, v99\n\t" \
    "v_exp_f32 v100, v100\n\t" \
    "v_mfma_f32_32x32x16_bf16 v[80:95], v[170:173], v[160:163], v[144:159]\n\t" \
    "ds_read_b64_tr_b16 v[170:171], v200 offset:16640\n\t" \
    "v_exp_f32 v101, v101\n\t" \
    "ds_read_b64_tr_b16 v[172:173], v200 offset:16704\n\t" \
    "v_exp_f32 v102, v102\n\t" \
    "v_exp_f32 v103, v103\n\t" \
    "v_cvt_pk_bf16_f32 v192, v96, v97\n\t" \
    "v_cvt_pk_bf16_f32 v193, v98, v99\n\t" \
    "v_cvt_pk_bf16_f32 v194, v100, v101\n\t" \
    "v_cvt_pk_bf16_f32 v195, v102, v103\n\t" \
    "v_exp_f32 v104, v104\n\t" \
    "v_exp_f32 v105, v105\n\t" \
    "v_exp_f32 v106, v106\n\t" \
    "v_exp_f32 v107, v107\n\t" \
    "v_mfma_f32_32x32x4_2b_bf16 v[64:95], v[174:175], v[164:165], v[64:95]\n\t" \
    "ds_read_b64_tr_b16 v[174:175], v201 offset:15360\n\t" \
    "v_exp_f32 v108, v108\n\t" \
    "v_exp_f32 v109, v109\n\t" \
    "v_exp_f32 v110, v110\n\t" \
    "v_exp_f32 v111, v111\n\t" \
    "v_cvt_pk_bf16_f32 v196, v104, v105\n\t" \
    "v_cvt_pk_bf16_f32 v197, v106, v107\n\t" \
    "v_cvt_pk_bf16_f32 v198, v108, v109\n\t" \
    "v_cvt_pk_bf16_f32 v199, v110, v111\n\t" \
    "v_exp_f32 v112, v112\n\t" \
    "s_setprio 0\n\t" \
    "s_waitcnt lgkmcnt(3)\n\t" \
    "v_exp_f32 v113, v113\n\t" \
    "v_mfma_f32_32x32x16_bf16 v[128:143], v[176:179], v[192:195], v[128:143]\n\t" \
    "v_exp_f32 v114, v114\n\t" \
    "v_exp_f32 v115, v115\n\t" \
    "v_exp_f32 v116, v116\n\t" \
    "v_exp_f32 v117, v117\n\t" \
    "v_mfma_f32_32x32x16_bf16 v[128:143], v[180:183], v[196:199], v[128:143]\n\t" \
    "v_exp_f32 v118, v118\n\t" \
    "v_exp_f32 v119, v119\n\t" \
    "v_cvt_pk_bf16_f32 v192, v112, v113\n\t" \
    "v_cvt_pk_bf16_f32 v193, v114, v115\n\t" \
    "v_cvt_pk_bf16_f32 v194, v116, v117\n\t" \
    "v_cvt_pk_bf16_f32 v195, v118, v119\n\t" \
    "v_exp_f32 v120, v120\n\t" \
    "v_exp_f32 v121, v121\n\t" \
    "v_mfma_f32_32x32x16_bf16 v[128:143], v[184:187], v[192:195], v[128:143]\n\t" \
    "v_exp_f32 v122, v122\n\t" \
    "v_exp_f32 v123, v123\n\t" \
    "v_exp_f32 v124, v124\n\t" \
    "v_exp_f32 v125, v125\n\t" \
    "v_exp_f32 v126, v126\n\t" \
    "v_exp_f32 v127, v127\n\t" \
    "v_cvt_pk_bf16_f32 v196, v120, v121\n\t" \
    "v_cvt_pk_bf16_f32 v197, v122, v123\n\t" \
    "v_cvt_pk_bf16_f32 v198, v124, v125\n\t" \
    "v_cvt_pk_bf16_f32 v199, v126, v127\n\t" \
    "s_setprio 3\n\t" \
    "s_waitcnt lgkmcnt(0)\n\t" \
    "v_mfma_f32_32x32x16_bf16 v[96:111], v[166:169], v[160:163], v[144:159]\n\t" \
    "ds_read_b64_tr_b16 v[166:167], v200 offset:20480\n\t" \
    "v_exp_f32 v64, v64\n\t" \
    "ds_read_b64_tr_b16 v[168:169], v200 offset:20544\n\t" \
    "v_exp_f32 v65, v65\n\t" \
    "v_mfma_f32_32x32x16_bf16 v[128:143], v[188:191], v[196:199], v[128:143]\n\t" \
    "s_mov_b64 exec, %[vmask]\n\t" \
    "ds_read_b128 v[176:179], v202 offset:10240\n\t" \
    "ds_read_b128 v[180:183], v202 offset:10880\n\t" \
    "ds_read_b128 v[184:187], v202 offset:11520\n\t" \
    "ds_read_b128 v[188:191], v202 offset:12160\n\t" \
    "s_mov_b64 exec, -1\n\t" \
    "v_exp_f32 v66, v66\n\t" \
    "v_exp_f32 v67, v67\n\t" \
    "v_exp_f32 v68, v68\n\t" \
    "v_mfma_f32_32x32x16_bf16 v[112:127], v[170:173], v[160:163], v[144:159]\n\t" \
    "ds_read_b64_tr_b16 v[170:171], v200 offset:21760\n\t" \
    "v_exp_f32 v69, v69\n\t" \
    "ds_read_b64_tr_b16 v[172:173], v200 offset:21824\n\t" \
    "v_exp_f32 v70, v70\n\t" \
    "v_exp_f32 v71, v71\n\t" \
    "v_cvt_pk_bf16_f32 v192, v64, v65\n\t" \
    "v_cvt_pk_bf16_f32 v193, v66, v67\n\t" \
    "v_cvt_pk_bf16_f32 v194, v68, v69\n\t" \
    "v_cvt_pk_bf16_f32 v195, v70, v71\n\t" \
    "v_exp_f32 v72, v72\n\t" \
    "v_exp_f32 v73, v73\n\t" \
    "v_exp_f32 v74, v74\n\t" \
    "v_exp_f32 v75, v75\n\t" \
    "v_mfma_f32_32x32x4_2b_bf16 v[96:127], v[174:175], v[164:165], v[96:127]\n\t" \
    "ds_read_b64_tr_b16 v[174:175], v201 offset:20480\n\t" \
    "v_exp_f32 v76, v76\n\t" \
    "v_exp_f32 v77, v77\n\t" \
    "v_exp_f32 v78, v78\n\t" \
    "v_exp_f32 v79, v79\n\t" \
    "v_cvt_pk_bf16_f32 v196, v72, v73\n\t" \
    "v_cvt_pk_bf16_f32 v197, v74, v75\n\t" \
    "v_cvt_pk_bf16_f32 v198, v76, v77\n\t" \
    "v_cvt_pk_bf16_f32 v199, v78, v79\n\t" \
    "v_exp_f32 v80, v80\n\t" \
    "s_setprio 2\n\t" \
    "s_waitcnt lgkmcnt(3)\n\t" \
    "v_exp_f32 v81, v81\n\t" \
    "v_mfma_f32_32x32x16_bf16 v[128:143], v[176:179], v[192:195], v[128:143]\n\t" \
    "v_exp_f32 v82, v82\n\t" \
    "v_exp_f32 v83, v83\n\t" \
    "v_exp_f32 v84, v84\n\t" \
    "v_exp_f32 v85, v85\n\t" \
    "v_mfma_f32_32x32x16_bf16 v[128:143], v[180:183], v[196:199], v[128:143]\n\t" \
    "v_exp_f32 v86, v86\n\t" \
    "v_exp_f32 v87, v87\n\t" \
    "v_cvt_pk_bf16_f32 v192, v80, v81\n\t" \
    "v_cvt_pk_bf16_f32 v193, v82, v83\n\t" \
    "v_cvt_pk_bf16_f32 v194, v84, v85\n\t" \
    "v_cvt_pk_bf16_f32 v195, v86, v87\n\t" \
    "v_exp_f32 v88, v88\n\t" \
    "v_exp_f32 v89, v89\n\t" \
    "v_mfma_f32_32x32x16_bf16 v[128:143], v[184:187], v[192:195], v[128:143]\n\t" \
    "v_exp_f32 v90, v90\n\t" \
    "v_exp_f32 v91, v91\n\t" \
    "v_exp_f32 v92, v92\n\t" \
    "v_exp_f32 v93, v93\n\t" \
    "v_exp_f32 v94, v94\n\t" \
    "v_exp_f32 v95, v95\n\t" \
    "v_cvt_pk_bf16_f32 v196, v88, v89\n\t" \
    "v_cvt_pk_bf16_f32 v197, v90, v91\n\t" \
    "v_cvt_pk_bf16_f32 v198, v92, v93\n\t" \
    "v_cvt_pk_bf16_f32 v199, v94, v95\n\t" \
    "s_setprio 1\n\t" \
    "s_waitcnt lgkmcnt(0)\n\t" \
    "v_mfma_f32_32x32x16_bf16 v[64:79], v[166:169], v[160:163], v[144:159]\n\t" \
    "ds_read_b64_tr_b16 v[166:167], v200 offset:25600\n\t" \
    "v_exp_f32 v96, v96\n\t" \
    "ds_read_b64_tr_b16 v[168:169], v200 offset:25664\n\t" \
    "v_exp_f32 v97, v97\n\t" \
    "v_mfma_f32_32x32x16_bf16 v[128:143], v[188:191], v[196:199], v[128:143]\n\t" \
    "s_mov_b64 exec, %[vmask]\n\t" \
    "ds_read_b128 v[176:179], v202 offset:15360\n\t" \
    "ds_read_b128 v[180:183], v202 offset:16000\n\t" \
    "ds_read_b128 v[184:187], v202 offset:16640\n\t" \
    "ds_read_b128 v[188:191], v202 offset:17280\n\t" \
    "s_mov_b64 exec, -1\n\t" \
    "v_exp_f32 v98, v98\n\t" \
    "v_exp_f32 v99, v99\n\t" \
    "v_exp_f32 v100, v100\n\t" \
    "v_mfma_f32_32x32x16_bf16 v[80:95], v[170:173], v[160:163], v[144:159]\n\t" \
    "ds_read_b64_tr_b16 v[170:171], v200 offset:26880\n\t" \
    "v_exp_f32 v101, v101\n\t" \
    "ds_read_b64_tr_b16 v[172:173], v200 offset:26944\n\t" \
    "v_exp_f32 v102, v102\n\t" \
    "v_exp_f32 v103, v103\n\t" \
    "v_cvt_pk_bf16_f32 v192, v96, v97\n\t" \
    "v_cvt_pk_bf16_f32 v193, v98, v99\n\t" \
    "v_cvt_pk_bf16_f32 v194, v100, v101\n\t" \
    "v_cvt_pk_bf16_f32 v195, v102, v103\n\t" \
    "v_exp_f32 v104, v104\n\t" \
    "v_exp_f32 v105, v105\n\t" \
    "v_exp_f32 v106, v106\n\t" \
    "v_exp_f32 v107, v107\n\t" \
    "v_mfma_f32_32x32x4_2b_bf16 v[64:95], v[174:175], v[164:165], v[64:95]\n\t" \
    "ds_read_b64_tr_b16 v[174:175], v201 offset:25600\n\t" \
    "v_exp_f32 v108, v108\n\t" \
    "v_exp_f32 v109, v109\n\t" \
    "v_exp_f32 v110, v110\n\t" \
    "v_exp_f32 v111, v111\n\t" \
    "v_cvt_pk_bf16_f32 v196, v104, v105\n\t" \
    "v_cvt_pk_bf16_f32 v197, v106, v107\n\t" \
    "v_cvt_pk_bf16_f32 v198, v108, v109\n\t" \
    "v_cvt_pk_bf16_f32 v199, v110, v111\n\t" \
    "v_exp_f32 v112, v112\n\t" \
    "s_setprio 0\n\t" \
    "s_waitcnt lgkmcnt(3)\n\t" \
    "v_exp_f32 v113, v113\n\t" \
    "v_mfma_f32_32x32x16_bf16 v[128:143], v[176:179], v[192:195], v[128:143]\n\t" \
    "v_exp_f32 v114, v114\n\t" \
    "v_exp_f32 v115, v115\n\t" \
    "v_exp_f32 v116, v116\n\t" \
    "v_exp_f32 v117, v117\n\t" \
    "v_mfma_f32_32x32x16_bf16 v[128:143], v[180:183], v[196:199], v[128:143]\n\t" \
    "v_exp_f32 v118, v118\n\t" \
    "v_exp_f32 v119, v119\n\t" \
    "v_cvt_pk_bf16_f32 v192, v112, v113\n\t" \
    "v_cvt_pk_bf16_f32 v193, v114, v115\n\t" \
    "v_cvt_pk_bf16_f32 v194, v116, v117\n\t" \
    "v_cvt_pk_bf16_f32 v195, v118, v119\n\t" \
    "v_exp_f32 v120, v120\n\t" \
    "v_exp_f32 v121, v121\n\t" \
    "v_mfma_f32_32x32x16_bf16 v[128:143], v[184:187], v[192:195], v[128:143]\n\t" \
    "v_exp_f32 v122, v122\n\t" \
    "v_exp_f32 v123, v123\n\t" \
    "v_exp_f32 v124, v124\n\t" \
    "v_exp_f32 v125, v125\n\t" \
    "v_exp_f32 v126, v126\n\t" \
    "v_exp_f32 v127, v127\n\t" \
    "v_cvt_pk_bf16_f32 v196, v120, v121\n\t" \
    "v_cvt_pk_bf16_f32 v197, v122, v123\n\t" \
    "v_cvt_pk_bf16_f32 v198, v124, v125\n\t" \
    "v_cvt_pk_bf16_f32 v199, v126, v127\n\t" \
    "v_add_u32 v200, 20480, v200\n\t" \
    "v_add_u32 v201, 20480, v201\n\t" \
    "v_add_u32 v202, 20480, v202\n\t" \
    "LH%=:\n\t" \
    "s_cmp_lg_u32 %[cnt], 2\n\t" \
    "s_cbranch_scc1 LP%=\n\t" \
    "s_waitcnt vmcnt(0)\n\t" \
    "s_barrier\n\t" \
    "s_cmp_lg_u32 %[wid], 0\n\t" \
    "s_cbranch_scc1 LP%=\n\t" \
    "v_mov_b32 v220, %[tgl]\n\t" \
    "v_mov_b32 v221, %[tgh]\n\t" \
    "s_mov_b64 exec, 1\n\t" \
    "buffer_store_dwordx2 v[220:221], %[rtf], %[wr], 0 offen sc1\n\t" \
    "s_mov_b64 exec, -1\n\t" \
    "LP%=:\n\t" \
    "s_sub_u32 %[cnt], %[cnt], 1\n\t" \
    "s_cmp_lg_u32 %[cnt], 0\n\t" \
    "s_cbranch_scc1 LB%=\n\t" \
    "buffer_load_dwordx4 v[208:211], %[rtc], %[wr], 0 offen sc1\n\t" \
    "s_setprio 3\n\t" \
    "s_waitcnt lgkmcnt(0)\n\t" \
    "v_mfma_f32_32x32x16_bf16 v[96:111], v[166:169], v[160:163], v[144:159]\n\t" \
    "ds_read_b64_tr_b16 v[166:167], v200 offset:10240\n\t" \
    "v_exp_f32 v64, v64\n\t" \
    "ds_read_b64_tr_b16 v[168:169], v200 offset:10304\n\t" \
    "v_exp_f32 v65, v65\n\t" \
    "v_mfma_f32_32x32x16_bf16 v[128:143], v[188:191], v[196:199], v[128:143]\n\t" \
    "s_mov_b64 exec, %[vmask]\n\t" \
    "ds_read_b128 v[176:179], v202 offset:0\n\t" \
    "ds_read_b128 v[180:183], v202 offset:640\n\t" \
    "ds_read_b128 v[184:187], v202 offset:1280\n\t" \
    "ds_read_b128 v[188:191], v202 offset:1920\n\t" \
    "s_mov_b64 exec, -1\n\t" \
    "v_exp_f32 v66, v66\n\t" \
    "v_exp_f32 v67, v67\n\t" \
    "v_exp_f32 v68, v68\n\t" \
    "v_mfma_f32_32x32x16_bf16 v[112:127], v[170:173], v[160:163], v[144:159]\n\t" \
    "ds_read_b64_tr_b16 v[170:171], v200 offset:11520\n\t" \
    "v_exp_f32 v69, v69\n\t" \
    "ds_read_b64_tr_b16 v[172:173], v200 offset:11584\n\t" \
    "v_exp_f32 v70, v70\n\t" \
    "v_exp_f32 v71, v71\n\t" \
    "v_cvt_pk_bf16_f32 v192, v64, v65\n\t" \
    "v_cvt_pk_bf16_f32 v193, v66, v67\n\t" \
    "v_cvt_pk_bf16_f32 v194, v68, v69\n\t" \
    "v_cvt_pk_bf16_f32 v195, v70, v71\n\t" \
    "v_exp_f32 v72, v72\n\t" \
    "v_exp_f32 v73, v73\n\t" \
    "v_exp_f32 v74, v74\n\t" \
    "v_exp_f32 v75, v75\n\t" \
    "v_mfma_f32_32x32x4_2b_bf16 v[96:127], v[174:175], v[164:165], v[96:127]\n\t" \
    "ds_read_b64_tr_b16 v[174:175], v201 offset:10240\n\t" \
    "v_exp_f32 v76, v76\n\t" \
    "v_exp_f32 v77, v77\n\t" \
    "v_exp_f32 v78, v78\n\t" \
    "v_exp_f32 v79, v79\n\t" \
    "v_cvt_pk_bf16_f32 v196, v72, v73\n\t" \
    "v_cvt_pk_bf16_f32 v197, v74, v75\n\t" \
    "v_cvt_pk_bf16_f32 v198, v76, v77\n\t" \
    "v_cvt_pk_bf16_f32 v199, v78, v79\n\t" \
    "v_exp_f32 v80, v80\n\t" \
    "s_setprio 2\n\t" \
    "s_waitcnt lgkmcnt(3)\n\t" \
    "v_exp_f32 v81, v81\n\t" \
    "v_mfma_f32_32x32x16_bf16 v[128:143], v[176:179], v[192:195], v[128:143]\n\t" \
    "v_exp_f32 v82, v82\n\t" \
    "v_exp_f32 v83, v83\n\t" \
    "v_exp_f32 v84, v84\n\t" \
    "v_exp_f32 v85, v85\n\t" \
    "v_mfma_f32_32x32x16_bf16 v[128:143], v[180:183], v[196:199], v[128:143]\n\t" \
    "v_exp_f32 v86, v86\n\t" \
    "v_exp_f32 v87, v87\n\t" \
    "v_cvt_pk_bf16_f32 v192, v80, v81\n\t" \
    "v_cvt_pk_bf16_f32 v193, v82, v83\n\t" \
    "v_cvt_pk_bf16_f32 v194, v84, v85\n\t" \
    "v_cvt_pk_bf16_f32 v195, v86, v87\n\t" \
    "v_exp_f32 v88, v88\n\t" \
    "v_exp_f32 v89, v89\n\t" \
    "v_mfma_f32_32x32x16_bf16 v[128:143], v[184:187], v[192:195], v[128:143]\n\t" \
    "v_exp_f32 v90, v90\n\t" \
    "v_exp_f32 v91, v91\n\t" \
    "v_exp_f32 v92, v92\n\t" \
    "v_exp_f32 v93, v93\n\t" \
    "v_exp_f32 v94, v94\n\t" \
    "v_exp_f32 v95, v95\n\t" \
    "v_cvt_pk_bf16_f32 v196, v88, v89\n\t" \
    "v_cvt_pk_bf16_f32 v197, v90, v91\n\t" \
    "v_cvt_pk_bf16_f32 v198, v92, v93\n\t" \
    "v_cvt_pk_bf16_f32 v199, v94, v95\n\t" \
    "s_setprio 1\n\t" \
    "s_waitcnt lgkmcnt(0)\n\t" \
    "v_mfma_f32_32x32x16_bf16 v[64:79], v[166:169], v[160:163], v[144:159]\n\t" \
    "ds_read_b64_tr_b16 v[166:167], v200 offset:15360\n\t" \
    "v_exp_f32 v96, v96\n\t" \
    "ds_read_b64_tr_b16 v[168:169], v200 offset:15424\n\t" \
    "v_exp_f32 v97, v97\n\t" \
    "v_mfma_f32_32x32x16_bf16 v[128:143], v[188:191], v[196:199], v[128:143]\n\t" \
    "s_mov_b64 exec, %[vmask]\n\t" \
    "ds_read_b128 v[176:179], v202 offset:5120\n\t" \
    "ds_read_b128 v[180:183], v202 offset:5760\n\t" \
    "ds_read_b128 v[184:187], v202 offset:6400\n\t" \
    "ds_read_b128 v[188:191], v202 offset:7040\n\t" \
    "s_mov_b64 exec, -1\n\t" \
    "v_exp_f32 v98, v98\n\t" \
    "v_exp_f32 v99, v99\n\t" \
    "v_exp_f32 v100, v100\n\t" \
    "v_mfma_f32_32x32x16_bf16 v[80:95], v[170:173], v[160:163], v[144:159]\n\t" \
    "ds_read_b64_tr_b16 v[170:171], v200 offset:16640\n\t" \
    "v_exp_f32 v101, v101\n\t" \
    "ds_read_b64_tr_b16 v[172:173], v200 offset:16704\n\t" \
    "v_exp_f32 v102, v102\n\t" \
    "v_exp_f32 v103, v103\n\t" \
    "v_cvt_pk_bf16_f32 v192, v96, v97\n\t" \
    "v_cvt_pk_bf16_f32 v193, v98, v99\n\t" \
    "v_cvt_pk_bf16_f32 v194, v100, v101\n\t" \
    "v_cvt_pk_bf16_f32 v195, v102, v103\n\t" \
    "v_exp_f32 v104, v104\n\t" \
    "v_exp_f32 v105, v105\n\t" \
    "v_exp_f32 v106, v106\n\t" \
    "v_exp_f32 v107, v107\n\t" \
    "v_mfma_f32_32x32x4_2b_bf16 v[64:95], v[174:175], v[164:165], v[64:95]\n\t" \
    "ds_read_b64_tr_b16 v[174:175], v201 offset:15360\n\t" \
    "v_exp_f32 v108, v108\n\t" \
    "v_exp_f32 v109, v109\n\t" \
    "v_exp_f32 v110, v110\n\t" \
    "v_exp_f32 v111, v111\n\t" \
    "v_cvt_pk_bf16_f32 v196, v104, v105\n\t" \
    "v_cvt_pk_bf16_f32 v197, v106, v107\n\t" \
    "v_cvt_pk_bf16_f32 v198, v108, v109\n\t" \
    "v_cvt_pk_bf16_f32 v199, v110, v111\n\t" \
    "v_exp_f32 v112, v112\n\t" \
    "s_setprio 0\n\t" \
    "s_waitcnt lgkmcnt(3)\n\t" \
    "v_exp_f32 v113, v113\n\t" \
    "v_mfma_f32_32x32x16_bf16 v[128:143], v[176:179], v[192:195], v[128:143]\n\t" \
    "v_exp_f32 v114, v114\n\t" \
    "v_exp_f32 v115, v115\n\t" \
    "v_exp_f32 v116, v116\n\t" \
    "v_exp_f32 v117, v117\n\t" \
    "v_mfma_f32_32x32x16_bf16 v[128:143], v[180:183], v[196:199], v[128:143]\n\t" \
    "v_exp_f32 v118, v118\n\t" \
    "v_exp_f32 v119, v119\n\t" \
    "v_cvt_pk_bf16_f32 v192, v112, v113\n\t" \
    "v_cvt_pk_bf16_f32 v193, v114, v115\n\t" \
    "v_cvt_pk_bf16_f32 v194, v116, v117\n\t" \
    "v_cvt_pk_bf16_f32 v195, v118, v119\n\t" \
    "v_exp_f32 v120, v120\n\t" \
    "v_exp_f32 v121, v121\n\t" \
    "v_mfma_f32_32x32x16_bf16 v[128:143], v[184:187], v[192:195], v[128:143]\n\t" \
    "v_exp_f32 v122, v122\n\t" \
    "v_exp_f32 v123, v123\n\t" \
    "v_exp_f32 v124, v124\n\t" \
    "v_exp_f32 v125, v125\n\t" \
    "v_exp_f32 v126, v126\n\t" \
    "v_exp_f32 v127, v127\n\t" \
    "v_cvt_pk_bf16_f32 v196, v120, v121\n\t" \
    "v_cvt_pk_bf16_f32 v197, v122, v123\n\t" \
    "v_cvt_pk_bf16_f32 v198, v124, v125\n\t" \
    "v_cvt_pk_bf16_f32 v199, v126, v127\n\t" \
    "s_mov_b32 %[cnt], 60000\n\t" \
    "LS%=:\n\t" \
    "s_waitcnt vmcnt(0)\n\t" \
    "v_xor_b32 v208, %[tgl], v208\n\t" \
    "v_xor_b32 v209, %[tgh], v209\n\t" \
    "v_xor_b32 v210, %[tgl], v210\n\t" \
    "v_xor_b32 v211, %[tgh], v211\n\t" \
    "v_or3_b32 v208, v208, v209, v210\n\t" \
    "v_or_b32 v208, v208, v211\n\t" \
    "v_cmp_ne_u32 vcc, 0, v208\n\t" \
    "s_nop 4\n\t" \
    "s_cbranch_vccz LT%=\n\t" \
    "buffer_load_dwordx4 v[208:211], %[rtc], %[wr], 0 offen sc1\n\t" \
    "s_sub_u32 %[cnt], %[cnt], 1\n\t" \
    "s_cmp_lg_u32 %[cnt], 0\n\t" \
    "s_cbranch_scc1 LS%=\n\t" \
    "s_waitcnt vmcnt(0)\n\t" \
    "LT%=:\n\t" \
    "s_barrier\n\t" \
    "s_mov_b32 m0, %[ldsw]\n\t" \
    "v_mov_b32 v203, %[low]\n\t" \
    "s_setprio 3\n\t" \
    "s_waitcnt lgkmcnt(0)\n\t" \
    "v_mfma_f32_32x32x16_bf16 v[96:111], v[166:169], v[160:163], v[144:159]\n\t" \
    "ds_read_b64_tr_b16 v[166:167], v200 offset:20480\n\t" \
    "v_exp_f32 v64, v64\n\t" \
    "ds_read_b64_tr_b16 v[168:169], v200 offset:20544\n\t" \
    "v_exp_f32 v65, v65\n\t" \
    "v_mfma_f32_32x32x16_bf16 v[128:143], v[188:191], v[196:199], v[128:143]\n\t" \
    "s_mov_b64 exec, %[vmask]\n\t" \
    "ds_read_b128 v[176:179], v202 offset:10240\n\t" \
    "ds_read_b128 v[180:183], v202 offset:10880\n\t" \
    "ds_read_b128 v[184:187], v202 offset:11520\n\t" \
    "ds_read_b128 v[188:191], v202 offset:12160\n\t" \
    "s_mov_b64 exec, -1\n\t" \
    "v_exp_f32 v66, v66\n\t" \
    "v_exp_f32 v67, v67\n\t" \
    "v_exp_f32 v68, v68\n\t" \
    "v_mfma_f32_32x32x16_bf16 v[112:127], v[170:173], v[160:163], v[144:159]\n\t" \
    "ds_read_b64_tr_b16 v[170:171], v200 offset:21760\n\t" \
    "v_exp_f32 v69, v69\n\t" \
    "ds_read_b64_tr_b16 v[172:173], v200 offset:21824\n\t" \
    "v_exp_f32 v70, v70\n\t" \
    "s_add_u32 m0, m0, 0x2000\n\t" \
    "v_add_u32 v203, 0x2000, v203\n\t" \
    "global_load_lds_dwordx4 v203, %[wo] sc1\n\t" \
    "s_add_u32 m0, m0, 0x2000\n\t" \
    "v_add_u32 v203, 0x2000, v203\n\t" \
    "global_load_lds_dwordx4 v203, %[wo] sc1\n\t" \
    "v_exp_f32 v71, v71\n\t" \
    "v_cvt_pk_bf16_f32 v192, v64, v65\n\t" \
    "v_cvt_pk_bf16_f32 v193, v66, v67\n\t" \
    "v_cvt_pk_bf16_f32 v194, v68, v69\n\t" \
    "v_cvt_pk_bf16_f32 v195, v70, v71\n\t" \
    "v_exp_f32 v72, v72\n\t" \
    "v_exp_f32 v73, v73\n\t" \
    "s_add_u32 m0, m0, 0x2000\n\t" \
    "v_add_u32 v203, 0x2000, v203\n\t" \
    "global_load_lds_dwordx4 v203, %[wo] sc1\n\t" \
    "v_exp_f32 v74, v74\n\t" \
    "v_exp_f32 v75, v75\n\t" \
    "v_mfma_f32_32x32x4_2b_bf16 v[96:127], v[174:175], v[164:165], v[96:127]\n\t" \
    "ds_read_b64_tr_b16 v[174:175], v201 offset:20480\n\t" \
    "v_exp_f32 v76, v76\n\t" \
    "v_exp_f32 v77, v77\n\t" \
    "s_add_u32 m0, m0, 0x2000\n\t" \
    "v_add_u32 v203, 0x2000, v203\n\t" \
    "global_load_lds_dwordx4 v203, %[wo] sc1\n\t" \
    "v_exp_f32 v78, v78\n\t" \
    "v_exp_f32 v79, v79\n\t" \
    "v_cvt_pk_bf16_f32 v196, v72, v73\n\t" \
    "v_cvt_pk_bf16_f32 v197, v74, v75\n\t" \
    "v_cvt_pk_bf16_f32 v198, v76, v77\n\t" \
    "v_cvt_pk_bf16_f32 v199, v78, v79\n\t" \
    "v_exp_f32 v80, v80\n\t" \
    "s_setprio 2\n\t" \
    "s_waitcnt lgkmcnt(3)\n\t" \
    "v_exp_f32 v81, v81\n\t" \
    "v_mfma_f32_32x32x16_bf16 v[128:143], v[176:179], v[192:195], v[128:143]\n\t" \
    "v_exp_f32 v82, v82\n\t" \
    "v_exp_f32 v83, v83\n\t" \
    "s_add_u32 m0, m0, 0x2000\n\t" \
    "v_add_u32 v203, 0x2000, v203\n\t" \
    "global_load_lds_dwordx4 v203, %[wo] sc1\n\t" \
    "v_exp_f32 v84, v84\n\t" \
    "v_exp_f32 v85, v85\n\t" \
    "v_mfma_f32_32x32x16_bf16 v[128:143], v[180:183], v[196:199], v[128:143]\n\t" \
    "v_exp_f32 v86, v86\n\t" \
    "v_exp_f32 v87, v87\n\t" \
    "v_cvt_pk_bf16_f32 v192, v80, v81\n\t" \
    "v_cvt_pk_bf16_f32 v193, v82, v83\n\t" \
    "v_cvt_pk_bf16_f32 v194, v84, v85\n\t" \
    "v_cvt_pk_bf16_f32 v195, v86, v87\n\t" \
    "v_exp_f32 v88, v88\n\t" \
    "v_exp_f32 v89, v89\n\t" \
    "v_mfma_f32_32x32x16_bf16 v[128:143], v[184:187], v[192:195], v[128:143]\n\t" \
    "v_exp_f32 v90, v90\n\t" \
    "v_exp_f32 v91, v91\n\t" \
    "s_add_u32 m0, m0, 0x2000\n\t" \
    "v_add_u32 v203, 0x2000, v203\n\t" \
    "global_load_lds_dwordx4 v203, %[wo] sc1\n\t" \
    "v_exp_f32 v92, v92\n\t" \
    "v_exp_f32 v93, v93\n\t" \
    "s_add_u32 m0, m0, 0x2000\n\t" \
    "v_add_u32 v203, 0x2000, v203\n\t" \
    "global_load_lds_dwordx4 v203, %[wo] sc1\n\t" \
    "v_exp_f32 v94, v94\n\t" \
    "v_exp_f32 v95, v95\n\t" \
    "v_cvt_pk_bf16_f32 v196, v88, v89\n\t" \
    "v_cvt_pk_bf16_f32 v197, v90, v91\n\t" \
    "v_cvt_pk_bf16_f32 v198, v92, v93\n\t" \
    "v_cvt_pk_bf16_f32 v199, v94, v95\n\t" \
    "s_setprio 1\n\t" \
    "s_waitcnt lgkmcnt(0)\n\t" \
    "v_mfma_f32_32x32x16_bf16 v[64:79], v[166:169], v[160:163], v[144:159]\n\t" \
    "ds_read_b64_tr_b16 v[166:167], v200 offset:25600\n\t" \
    "v_exp_f32 v96, v96\n\t" \
    "ds_read_b64_tr_b16 v[168:169], v200 offset:25664\n\t" \
    "v_exp_f32 v97, v97\n\t" \
    "v_mfma_f32_32x32x16_bf16 v[128:143], v[188:191], v[196:199], v[128:143]\n\t" \
    "s_mov_b64 exec, %[vmask]\n\t" \
    "ds_read_b128 v[176:179], v202 offset:15360\n\t" \
    "ds_read_b128 v[180:183], v202 offset:16000\n\t" \
    "ds_read_b128 v[184:187], v202 offset:16640\n\t" \
    "ds_read_b128 v[188:191], v202 offset:17280\n\t" \
    "s_mov_b64 exec, -1\n\t" \
    "v_exp_f32 v98, v98\n\t" \
    "v_exp_f32 v99, v99\n\t" \
    "v_exp_f32 v100, v100\n\t" \
    "v_mfma_f32_32x32x16_bf16 v[80:95], v[170:173], v[160:163], v[144:159]\n\t" \
    "ds_read_b64_tr_b16 v[170:171], v200 offset:26880\n\t" \
    "v_exp_f32 v101, v101\n\t" \
    "ds_read_b64_tr_b16 v[172:173], v200 offset:26944\n\t" \
    "v_exp_f32 v102, v102\n\t" \
    "s_add_u32 m0, m0, 0x2000\n\t" \
    "v_add_u32 v203, 0x2000, v203\n\t" \
    "global_load_lds_dwordx4 v203, %[wo] sc1\n\t" \
    "v_exp_f32 v103, v103\n\t" \
    "v_cvt_pk_bf16_f32 v192, v96, v97\n\t" \
    "v_cvt_pk_bf16_f32 v193, v98, v99\n\t" \
    "v_cvt_pk_bf16_f32 v194, v100, v101\n\t" \
    "v_cvt_pk_bf16_f32 v195, v102, v103\n\t" \
    "v_exp_f32 v104, v104\n\t" \
    "v_exp_f32 v105, v105\n\t" \
    "s_add_u32 m0, m0, 0x2000\n\t" \
    "v_add_u32 v203, 0x2000, v203\n\t" \
    "global_load_lds_dwordx4 v203, %[wo] sc1\n\t" \
    "v_exp_f32 v106, v106\n\t" \
    "v_exp_f32 v107, v107\n\t" \
    "v_mfma_f32_32x32x4_2b_bf16 v[64:95], v[174:175], v[164:165], v[64:95]\n\t" \
    "ds_read_b64_tr_b16 v[174:175], v201 offset:25600\n\t" \
    "v_exp_f32 v108, v108\n\t" \
    "v_exp_f32 v109, v109\n\t" \
    "s_add_u32 m0, m0, 0x2000\n\t" \
    "v_add_u32 v203, 0x2000, v203\n\t" \
    "global_load_lds_dwordx4 v203, %[wo] sc1\n\t" \
    "v_exp_f32 v110, v110\n\t" \
    "v_exp_f32 v111, v111\n\t" \
    "v_cvt_pk_bf16_f32 v196, v104, v105\n\t" \
    "v_cvt_pk_bf16_f32 v197, v106, v107\n\t" \
    "v_cvt_pk_bf16_f32 v198, v108, v109\n\t" \
    "v_cvt_pk_bf16_f32 v199, v110, v111\n\t" \
    "v_exp_f32 v112, v112\n\t" \
    "s_setprio 0\n\t" \
    "s_waitcnt lgkmcnt(3)\n\t" \
    "v_exp_f32 v113, v113\n\t" \
    "v_mfma_f32_32x32x16_bf16 v[128:143], v[176:179], v[192:195], v[128:143]\n\t" \
    "v_exp_f32 v114, v114\n\t" \
    "v_exp_f32 v115, v115\n\t" \
    "s_add_u32 m0, m0, 0x2000\n\t" \
    "v_add_u32 v203, 0x2000, v203\n\t" \
    "global_load_lds_dwordx4 v203, %[wo] sc1\n\t" \
    "v_exp_f32 v116, v116\n\t" \
    "v_exp_f32 v117, v117\n\t" \
    "v_mfma_f32_32x32x16_bf16 v[128:143], v[180:183], v[196:199], v[128:143]\n\t" \
    "v_exp_f32 v118, v118\n\t" \
    "v_exp_f32 v119, v119\n\t" \
    "v_cvt_pk_bf16_f32 v192, v112, v113\n\t" \
    "v_cvt_pk_bf16_f32 v193, v114, v115\n\t" \
    "v_cvt_pk_bf16_f32 v194, v116, v117\n\t" \
    "v_cvt_pk_bf16_f32 v195, v118, v119\n\t" \
    "v_exp_f32 v120, v120\n\t" \
    "v_exp_f32 v121, v121\n\t" \
    "v_mfma_f32_32x32x16_bf16 v[128:143], v[184:187], v[192:195], v[128:143]\n\t" \
    "v_exp_f32 v122, v122\n\t" \
    "v_exp_f32 v123, v123\n\t" \
    "s_add_u32 m0, m0, 0x2000\n\t" \
    "v_add_u32 v203, 0x2000, v203\n\t" \
    "global_load_lds_dwordx4 v203, %[wo] sc1\n\t" \
    "v_exp_f32 v124, v124\n\t" \
    "v_exp_f32 v125, v125\n\t" \
    "s_add_u32 m0, m0, 0x2000\n\t" \
    "v_add_u32 v203, 0x2000, v203\n\t" \
    "global_load_lds_dwordx4 v203, %[wo] sc1\n\t" \
    "v_exp_f32 v126, v126\n\t" \
    "v_exp_f32 v127, v127\n\t" \
    "v_cvt_pk_bf16_f32 v196, v120, v121\n\t" \
    "v_cvt_pk_bf16_f32 v197, v122, v123\n\t" \
    "v_cvt_pk_bf16_f32 v198, v124, v125\n\t" \
    "v_cvt_pk_bf16_f32 v199, v126, v127\n\t" \
    "s_setprio 0\n\t" \
    "s_nop 1\n\t" \
    "v_mfma_f32_32x32x16_bf16 v[128:143], v[188:191], v[196:199], v[128:143]\n\t" \
    "s_waitcnt lgkmcnt(0)\n\t" \
    "s_nop 15\n\t" \
    "v_mov_b32 %[o0], v128\n\t" \
    "v_mov_b32 %[o1], v129\n\t" \
    "v_mov_b32 %[o2], v130\n\t" \
    "v_mov_b32 %[o3], v131\n\t" \
    "v_mov_b32 %[o4], v132\n\t" \
    "v_mov_b32 %[o5], v133\n\t" \
    "v_mov_b32 %[o6], v134\n\t" \
    "v_mov_b32 %[o7], v135\n\t" \
    "v_mov_b32 %[o8], v136\n\t" \
    "v_mov_b32 %[o9], v137\n\t" \
    "v_mov_b32 %[o10], v138\n\t" \
    "v_mov_b32 %[o11], v139\n\t" \
    "s_branch LZ%=\n\t" \
    "LX%=:\n\t" \
    "s_waitcnt vmcnt(0)\n\t" \
    "s_setprio 3\n\t" \
    "s_waitcnt lgkmcnt(0)\n\t" \
    "v_mfma_f32_32x32x16_bf16 v[96:111], v[166:169], v[160:163], v[144:159]\n\t" \
    "ds_read_b64_tr_b16 v[166:167], v200 offset:10240\n\t" \
    "v_exp_f32 v64, v64\n\t" \
    "ds_read_b64_tr_b16 v[168:169], v200 offset:10304\n\t" \
    "v_exp_f32 v65, v65\n\t" \
    "v_mfma_f32_32x32x16_bf16 v[128:143], v[188:191], v[196:199], v[128:143]\n\t" \
    "s_mov_b64 exec, %[vmask]\n\t" \
    "ds_read_b128 v[176:179], v202 offset:0\n\t" \
    "ds_read_b128 v[180:183], v202 offset:640\n\t" \
    "ds_read_b128 v[184:187], v202 offset:1280\n\t" \
    "ds_read_b128 v[188:191], v202 offset:1920\n\t" \
    "s_mov_b64 exec, -1\n\t" \
    "v_exp_f32 v66, v66\n\t" \
    "v_exp_f32 v67, v67\n\t" \
    "v_perm_b32 v220, v205, v204, %[sel0]\n\t" \
    "v_perm_b32 v224, v205, v204, %[sel1]\n\t" \
    "v_perm_b32 v221, v207, v206, %[sel0]\n\t" \
    "v_perm_b32 v225, v207, v206, %[sel1]\n\t" \
    "v_perm_b32 v222, v209, v208, %[sel0]\n\t" \
    "v_perm_b32 v226, v209, v208, %[sel1]\n\t" \
    "v_perm_b32 v223, v211, v210, %[sel0]\n\t" \
    "v_perm_b32 v227, v211, v210, %[sel1]\n\t" \
    "v_mov_b32 v204, v220\n\t" \
    "v_mov_b32 v205, v221\n\t" \
    "v_mov_b32 v206, v222\n\t" \
    "v_mov_b32 v207, v223\n\t" \
    "v_mov_b32 v208, v224\n\t" \
    "v_mov_b32 v209, v225\n\t" \
    "v_mov_b32 v210, v226\n\t" \
    "v_mov_b32 v211, v227\n\t" \
    "v_mov_b32_dpp v204, v224 row_ror:8 row_mask:0xf bank_mask:0xc\n\t" \
    "v_mov_b32_dpp v205, v225 row_ror:8 row_mask:0xf bank_mask:0xc\n\t" \
    "v_mov_b32_dpp v206, v226 row_ror:8 row_mask:0xf bank_mask:0xc\n\t" \
    "v_mov_b32_dpp v207, v227 row_ror:8 row_mask:0xf bank_mask:0xc\n\t" \
    "v_mov_b32_dpp v208, v220 row_ror:8 row_mask:0xf bank_mask:0x3\n\t" \
    "v_mov_b32_dpp v209, v221 row_ror:8 row_mask:0xf bank_mask:0x3\n\t" \
    "v_mov_b32_dpp v210, v222 row_ror:8 row_mask:0xf bank_mask:0x3\n\t" \
    "v_mov_b32_dpp v211, v223 row_ror:8 row_mask:0xf bank_mask:0x3\n\t" \
    "buffer_store_dwordx4 v[204:207], %[rd0], %[wr], 0 offen sc1\n\t" \
    "v_exp_f32 v68, v68\n\t" \
    "v_mfma_f32_32x32x16_bf16 v[112:127], v[170:173], v[160:163], v[144:159]\n\t" \
    "ds_read_b64_tr_b16 v[170:171], v200 offset:11520\n\t" \
    "v_exp_f32 v69, v69\n\t" \
    "ds_read_b64_tr_b16 v[172:173], v200 offset:11584\n\t" \
    "v_exp_f32 v70, v70\n\t" \
    "v_exp_f32 v71, v71\n\t" \
    "v_cvt_pk_bf16_f32 v192, v64, v65\n\t" \
    "v_cvt_pk_bf16_f32 v193, v66, v67\n\t" \
    "v_cvt_pk_bf16_f32 v194, v68, v69\n\t" \
    "v_cvt_pk_bf16_f32 v195, v70, v71\n\t" \
    "v_exp_f32 v72, v72\n\t" \
    "v_exp_f32 v73, v73\n\t" \
    "v_exp_f32 v74, v74\n\t" \
    "v_exp_f32 v75, v75\n\t" \
    "v_mfma_f32_32x32x4_2b_bf16 v[96:127], v[174:175], v[164:165], v[96:127]\n\t" \
    "ds_read_b64_tr_b16 v[174:175], v201 offset:10240\n\t" \
    "v_exp_f32 v76, v76\n\t" \
    "v_exp_f32 v77, v77\n\t" \
    "v_exp_f32 v78, v78\n\t" \
    "v_exp_f32 v79, v79\n\t" \
    "v_cvt_pk_bf16_f32 v196, v72, v73\n\t" \
    "v_cvt_pk_bf16_f32 v197, v74, v75\n\t" \
    "v_cvt_pk_bf16_f32 v198, v76, v77\n\t" \
    "v_cvt_pk_bf16_f32 v199, v78, v79\n\t" \
    "v_exp_f32 v80, v80\n\t" \
    "s_setprio 2\n\t" \
    "s_waitcnt lgkmcnt(3)\n\t" \
    "v_exp_f32 v81, v81\n\t" \
    "v_mfma_f32_32x32x16_bf16 v[128:143], v[176:179], v[192:195], v[128:143]\n\t" \
    "v_exp_f32 v82, v82\n\t" \
    "v_exp_f32 v83, v83\n\t" \
    "v_exp_f32 v84, v84\n\t" \
    "v_exp_f32 v85, v85\n\t" \
    "v_mfma_f32_32x32x16_bf16 v[128:143], v[180:183], v[196:199], v[128:143]\n\t" \
    "v_exp_f32 v86, v86\n\t" \
    "v_exp_f32 v87, v87\n\t" \
    "v_cvt_pk_bf16_f32 v192, v80, v81\n\t" \
    "v_cvt_pk_bf16_f32 v193, v82, v83\n\t" \
    "v_cvt_pk_bf16_f32 v194, v84, v85\n\t" \
    "v_cvt_pk_bf16_f32 v195, v86, v87\n\t" \
    "v_exp_f32 v88, v88\n\t" \
    "v_exp_f32 v89, v89\n\t" \
    "v_mfma_f32_32x32x16_bf16 v[128:143], v[184:187], v[192:195], v[128:143]\n\t" \
    "v_exp_f32 v90, v90\n\t" \
    "v_exp_f32 v91, v91\n\t" \
    "v_exp_f32 v92, v92\n\t" \
    "v_exp_f32 v93, v93\n\t" \
    "v_exp_f32 v94, v94\n\t" \
    "v_exp_f32 v95, v95\n\t" \
    "v_cvt_pk_bf16_f32 v196, v88, v89\n\t" \
    "v_cvt_pk_bf16_f32 v197, v90, v91\n\t" \
    "v_cvt_pk_bf16_f32 v198, v92, v93\n\t" \
    "v_cvt_pk_bf16_f32 v199, v94, v95\n\t" \
    "s_setprio 1\n\t" \
    "s_waitcnt lgkmcnt(0)\n\t" \
    "v_mfma_f32_32x32x16_bf16 v[64:79], v[166:169], v[160:163], v[144:159]\n\t" \
    "ds_read_b64_tr_b16 v[166:167], v200 offset:15360\n\t" \
    "v_exp_f32 v96, v96\n\t" \
    "ds_read_b64_tr_b16 v[168:169], v200 offset:15424\n\t" \
    "v_exp_f32 v97, v97\n\t" \
    "buffer_store_dwordx4 v[208:211], %[rd1], %[wr], 0 offen sc1\n\t" \
    "v_mfma_f32_32x32x16_bf16 v[128:143], v[188:191], v[196:199], v[128:143]\n\t" \
    "s_mov_b64 exec, %[vmask]\n\t" \
    "ds_read_b128 v[176:179], v202 offset:5120\n\t" \
    "ds_read_b128 v[180:183], v202 offset:5760\n\t" \
    "ds_read_b128 v[184:187], v202 offset:6400\n\t" \
    "ds_read_b128 v[188:191], v202 offset:7040\n\t" \
    "s_mov_b64 exec, -1\n\t" \
    "v_exp_f32 v98, v98\n\t" \
    "v_exp_f32 v99, v99\n\t" \
    "v_exp_f32 v100, v100\n\t" \
    "v_mfma_f32_32x32x16_bf16 v[80:95], v[170:173], v[160:163], v[144:159]\n\t" \
    "ds_read_b64_tr_b16 v[170:171], v200 offset:16640\n\t" \
    "v_exp_f32 v101, v101\n\t" \
    "ds_read_b64_tr_b16 v[172:173], v200 offset:16704\n\t" \
    "v_exp_f32 v102, v102\n\t" \
    "v_exp_f32 v103, v103\n\t" \
    "v_cvt_pk_bf16_f32 v192, v96, v97\n\t" \
    "v_cvt_pk_bf16_f32 v193, v98, v99\n\t" \
    "v_cvt_pk_bf16_f32 v194, v100, v101\n\t" \
    "v_cvt_pk_bf16_f32 v195, v102, v103\n\t" \
    "v_exp_f32 v104, v104\n\t" \
    "v_exp_f32 v105, v105\n\t" \
    "v_exp_f32 v106, v106\n\t" \
    "v_exp_f32 v107, v107\n\t" \
    "v_mfma_f32_32x32x4_2b_bf16 v[64:95], v[174:175], v[164:165], v[64:95]\n\t" \
    "ds_read_b64_tr_b16 v[174:175], v201 offset:15360\n\t" \
    "v_exp_f32 v108, v108\n\t" \
    "v_exp_f32 v109, v109\n\t" \
    "v_exp_f32 v110, v110\n\t" \
    "v_exp_f32 v111, v111\n\t" \
    "v_cvt_pk_bf16_f32 v196, v104, v105\n\t" \
    "v_cvt_pk_bf16_f32 v197, v106, v107\n\t" \
    "v_cvt_pk_bf16_f32 v198, v108, v109\n\t" \
    "v_cvt_pk_bf16_f32 v199, v110, v111\n\t" \
    "v_exp_f32 v112, v112\n\t" \
    "s_setprio 0\n\t" \
    "s_waitcnt lgkmcnt(3)\n\t" \
    "v_exp_f32 v113, v113\n\t" \
    "v_mfma_f32_32x32x16_bf16 v[128:143], v[176:179], v[192:195], v[128:143]\n\t" \
    "v_exp_f32 v114, v114\n\t" \
    "v_exp_f32 v115, v115\n\t" \
    "v_exp_f32 v116, v116\n\t" \
    "v_exp_f32 v117, v117\n\t" \
    "v_mfma_f32_32x32x16_bf16 v[128:143], v[180:183], v[196:199], v[128:143]\n\t" \
    "v_exp_f32 v118, v118\n\t" \
    "v_exp_f32 v119, v119\n\t" \
    "v_cvt_pk_bf16_f32 v192, v112, v113\n\t" \
    "v_cvt_pk_bf16_f32 v193, v114, v115\n\t" \
    "v_cvt_pk_bf16_f32 v194, v116, v117\n\t" \
    "v_cvt_pk_bf16_f32 v195, v118, v119\n\t" \
    "v_exp_f32 v120, v120\n\t" \
    "v_exp_f32 v121, v121\n\t" \
    "v_mfma_f32_32x32x16_bf16 v[128:143], v[184:187], v[192:195], v[128:143]\n\t" \
    "v_exp_f32 v122, v122\n\t" \
    "v_exp_f32 v123, v123\n\t" \
    "v_exp_f32 v124, v124\n\t" \
    "v_exp_f32 v125, v125\n\t" \
    "v_exp_f32 v126, v126\n\t" \
    "v_exp_f32 v127, v127\n\t" \
    "v_perm_b32 v220, v213, v212, %[sel0]\n\t" \
    "v_perm_b32 v224, v213, v212, %[sel1]\n\t" \
    "v_perm_b32 v221, v215, v214, %[sel0]\n\t" \
    "v_perm_b32 v225, v215, v214, %[sel1]\n\t" \
    "v_perm_b32 v222, v217, v216, %[sel0]\n\t" \
    "v_perm_b32 v226, v217, v216, %[sel1]\n\t" \
    "v_perm_b32 v223, v219, v218, %[sel0]\n\t" \
    "v_perm_b32 v227, v219, v218, %[sel1]\n\t" \
    "v_mov_b32 v212, v220\n\t" \
    "v_mov_b32 v213, v221\n\t" \
    "v_mov_b32 v214, v222\n\t" \
    "v_mov_b32 v215, v223\n\t" \
    "v_mov_b32 v216, v224\n\t" \
    "v_mov_b32 v217, v225\n\t" \
    "v_mov_b32 v218, v226\n\t" \
    "v_mov_b32 v219, v227\n\t" \
    "v_mov_b32_dpp v212, v224 row_ror:8 row_mask:0xf bank_mask:0xc\n\t" \
    "v_mov_b32_dpp v213, v225 row_ror:8 row_mask:0xf bank_mask:0xc\n\t" \
    "v_mov_b32_dpp v214, v226 row_ror:8 row_mask:0xf bank_mask:0xc\n\t" \
    "v_mov_b32_dpp v215, v227 row_ror:8 row_mask:0xf bank_mask:0xc\n\t" \
    "v_mov_b32_dpp v216, v220 row_ror:8 row_mask:0xf bank_mask:0x3\n\t" \
    "v_mov_b32_dpp v217, v221 row_ror:8 row_mask:0xf bank_mask:0x3\n\t" \
    "v_mov_b32_dpp v218, v222 row_ror:8 row_mask:0xf bank_mask:0x3\n\t" \
    "v_mov_b32_dpp v219, v223 row_ror:8 row_mask:0xf bank_mask:0x3\n\t" \
    "buffer_store_dwordx4 v[212:215], %[rd2], %[wr], 0 offen sc1\n\t" \
    "v_cvt_pk_bf16_f32 v196, v120, v121\n\t" \
    "v_cvt_pk_bf16_f32 v197, v122, v123\n\t" \
    "v_cvt_pk_bf16_f32 v198, v124, v125\n\t" \
    "v_cvt_pk_bf16_f32 v199, v126, v127\n\t" \
    "s_setprio 3\n\t" \
    "s_waitcnt lgkmcnt(0)\n\t" \
    "v_mfma_f32_32x32x16_bf16 v[96:111], v[166:169], v[160:163], v[144:159]\n\t" \
    "ds_read_b64_tr_b16 v[166:167], v200 offset:20480\n\t" \
    "v_exp_f32 v64, v64\n\t" \
    "ds_read_b64_tr_b16 v[168:169], v200 offset:20544\n\t" \
    "v_exp_f32 v65, v65\n\t" \
    "v_mfma_f32_32x32x16_bf16 v[128:143], v[188:191], v[196:199], v[128:143]\n\t" \
    "s_mov_b64 exec, %[vmask]\n\t" \
    "ds_read_b128 v[176:179], v202 offset:10240\n\t" \
    "ds_read_b128 v[180:183], v202 offset:10880\n\t" \
    "ds_read_b128 v[184:187], v202 offset:11520\n\t" \
    "ds_read_b128 v[188:191], v202 offset:12160\n\t" \
    "s_mov_b64 exec, -1\n\t" \
    "v_exp_f32 v66, v66\n\t" \
    "v_exp_f32 v67, v67\n\t" \
    "v_exp_f32 v68, v68\n\t" \
    "v_mfma_f32_32x32x16_bf16 v[112:127], v[170:173], v[160:163], v[144:159]\n\t" \
    "ds_read_b64_tr_b16 v[170:171], v200 offset:21760\n\t" \
    "v_exp_f32 v69, v69\n\t" \
    "ds_read_b64_tr_b16 v[172:173], v200 offset:21824\n\t" \
    "v_exp_f32 v70, v70\n\t" \
    "v_exp_f32 v71, v71\n\t" \
    "v_cvt_pk_bf16_f32 v192, v64, v65\n\t" \
    "v_cvt_pk_bf16_f32 v193, v66, v67\n\t" \
    "v_cvt_pk_bf16_f32 v194, v68, v69\n\t" \
    "v_cvt_pk_bf16_f32 v195, v70, v71\n\t" \
    "v_exp_f32 v72, v72\n\t" \
    "v_exp_f32 v73, v73\n\t" \
    "v_exp_f32 v74, v74\n\t" \
    "v_exp_f32 v75, v75\n\t" \
    "v_mfma_f32_32x32x4_2b_bf16 v[96:127], v[174:175], v[164:165], v[96:127]\n\t" \
    "ds_read_b64_tr_b16 v[174:175], v201 offset:20480\n\t" \
    "v_exp_f32 v76, v76\n\t" \
    "v_exp_f32 v77, v77\n\t" \
    "v_exp_f32 v78, v78\n\t" \
    "v_exp_f32 v79, v79\n\t" \
    "v_cvt_pk_bf16_f32 v196, v72, v73\n\t" \
    "v_cvt_pk_bf16_f32 v197, v74, v75\n\t" \
    "v_cvt_pk_bf16_f32 v198, v76, v77\n\t" \
    "v_cvt_pk_bf16_f32 v199, v78, v79\n\t" \
    "v_exp_f32 v80, v80\n\t" \
    "s_setprio 2\n\t" \
    "s_waitcnt lgkmcnt(3)\n\t" \
    "v_exp_f32 v81, v81\n\t" \
    "v_mfma_f32_32x32x16_bf16 v[128:143], v[176:179], v[192:195], v[128:143]\n\t" \
    "v_exp_f32 v82, v82\n\t" \
    "v_exp_f32 v83, v83\n\t" \
    "v_exp_f32 v84, v84\n\t" \
    "v_exp_f32 v85, v85\n\t" \
    "v_mfma_f32_32x32x16_bf16 v[128:143], v[180:183], v[196:199], v[128:143]\n\t" \
    "v_exp_f32 v86, v86\n\t" \
    "v_exp_f32 v87, v87\n\t" \
    "v_cvt_pk_bf16_f32 v192, v80, v81\n\t" \
    "v_cvt_pk_bf16_f32 v193, v82, v83\n\t" \
    "v_cvt_pk_bf16_f32 v194, v84, v85\n\t" \
    "v_cvt_pk_bf16_f32 v195, v86, v87\n\t" \
    "v_exp_f32 v88, v88\n\t" \
    "v_exp_f32 v89, v89\n\t" \
    "v_mfma_f32_32x32x16_bf16 v[128:143], v[184:187], v[192:195], v[128:143]\n\t" \
    "v_exp_f32 v90, v90\n\t" \
    "v_exp_f32 v91, v91\n\t" \
    "v_exp_f32 v92, v92\n\t" \
    "v_exp_f32 v93, v93\n\t" \
    "buffer_store_dwordx4 v[216:219], %[rd3], %[wr], 0 offen sc1\n\t" \
    "v_exp_f32 v94, v94\n\t" \
    "v_exp_f32 v95, v95\n\t" \
    "v_cvt_pk_bf16_f32 v196, v88, v89\n\t" \
    "v_cvt_pk_bf16_f32 v197, v90, v91\n\t" \
    "v_cvt_pk_bf16_f32 v198, v92, v93\n\t" \
    "v_cvt_pk_bf16_f32 v199, v94, v95\n\t" \
    "s_setprio 1\n\t" \
    "s_waitcnt lgkmcnt(0)\n\t" \
    "v_mfma_f32_32x32x16_bf16 v[64:79], v[166:169], v[160:163], v[144:159]\n\t" \
    "ds_read_b64_tr_b16 v[166:167], v200 offset:25600\n\t" \
    "v_exp_f32 v96, v96\n\t" \
    "ds_read_b64_tr_b16 v[168:169], v200 offset:25664\n\t" \
    "v_exp_f32 v97, v97\n\t" \
    "v_mfma_f32_32x32x16_bf16 v[128:143], v[188:191], v[196:199], v[128:143]\n\t" \
    "s_mov_b64 exec, %[vmask]\n\t" \
    "ds_read_b128 v[176:179], v202 offset:15360\n\t" \
    "ds_read_b128 v[180:183], v202 offset:16000\n\t" \
    "ds_read_b128 v[184:187], v202 offset:16640\n\t" \
    "ds_read_b128 v[188:191], v202 offset:17280\n\t" \
    "s_mov_b64 exec, -1\n\t" \
    "v_exp_f32 v98, v98\n\t" \
    "v_exp_f32 v99, v99\n\t" \
    "v_exp_f32 v100, v100\n\t" \
    "v_mfma_f32_32x32x16_bf16 v[80:95], v[170:173], v[160:163], v[144:159]\n\t" \
    "ds_read_b64_tr_b16 v[170:171], v200 offset:26880\n\t" \
    "v_exp_f32 v101, v101\n\t" \
    "ds_read_b64_tr_b16 v[172:173], v200 offset:26944\n\t" \
    "v_exp_f32 v102, v102\n\t" \
    "v_exp_f32 v103, v103\n\t" \
    "v_cvt_pk_bf16_f32 v192, v96, v97\n\t" \
    "v_cvt_pk_bf16_f32 v193, v98, v99\n\t" \
    "v_cvt_pk_bf16_f32 v194, v100, v101\n\t" \
    "v_cvt_pk_bf16_f32 v195, v102, v103\n\t" \
    "v_exp_f32 v104, v104\n\t" \
    "v_exp_f32 v105, v105\n\t" \
    "v_exp_f32 v106, v106\n\t" \
    "v_exp_f32 v107, v107\n\t" \
    "v_mfma_f32_32x32x4_2b_bf16 v[64:95], v[174:175], v[164:165], v[64:95]\n\t" \
    "ds_read_b64_tr_b16 v[174:175], v201 offset:25600\n\t" \
    "v_exp_f32 v108, v108\n\t" \
    "v_exp_f32 v109, v109\n\t" \
    "v_exp_f32 v110, v110\n\t" \
    "v_exp_f32 v111, v111\n\t" \
    "v_cvt_pk_bf16_f32 v196, v104, v105\n\t" \
    "v_cvt_pk_bf16_f32 v197, v106, v107\n\t" \
    "v_cvt_pk_bf16_f32 v198, v108, v109\n\t" \
    "v_cvt_pk_bf16_f32 v199, v110, v111\n\t" \
    "v_exp_f32 v112, v112\n\t" \
    "s_setprio 0\n\t" \
    "s_waitcnt lgkmcnt(3)\n\t" \
    "v_exp_f32 v113, v113\n\t" \
    "v_mfma_f32_32x32x16_bf16 v[128:143], v[176:179], v[192:195], v[128:143]\n\t" \
    "v_exp_f32 v114, v114\n\t" \
    "v_exp_f32 v115, v115\n\t" \
    "v_exp_f32 v116, v116\n\t" \
    "v_exp_f32 v117, v117\n\t" \
    "v_mfma_f32_32x32x16_bf16 v[128:143], v[180:183], v[196:199], v[128:143]\n\t" \
    "v_exp_f32 v118, v118\n\t" \
    "v_exp_f32 v119, v119\n\t" \
    "v_cvt_pk_bf16_f32 v192, v112, v113\n\t" \
    "v_cvt_pk_bf16_f32 v193, v114, v115\n\t" \
    "v_cvt_pk_bf16_f32 v194, v116, v117\n\t" \
    "v_cvt_pk_bf16_f32 v195, v118, v119\n\t" \
    "v_exp_f32 v120, v120\n\t" \
    "v_exp_f32 v121, v121\n\t" \
    "v_mfma_f32_32x32x16_bf16 v[128:143], v[184:187], v[192:195], v[128:143]\n\t" \
    "v_exp_f32 v122, v122\n\t" \
    "v_exp_f32 v123, v123\n\t" \
    "v_exp_f32 v124, v124\n\t" \
    "v_exp_f32 v125, v125\n\t" \
    "v_exp_f32 v126, v126\n\t" \
    "v_exp_f32 v127, v127\n\t" \
    "v_cvt_pk_bf16_f32 v196, v120, v121\n\t" \
    "v_cvt_pk_bf16_f32 v197, v122, v123\n\t" \
    "v_cvt_pk_bf16_f32 v198, v124, v125\n\t" \
    "v_cvt_pk_bf16_f32 v199, v126, v127\n\t" \
    "v_add_u32 v200, 20480, v200\n\t" \
    "v_add_u32 v201, 20480, v201\n\t" \
    "v_add_u32 v202, 20480, v202\n\t" \
    "s_branch LH%=\n\t" \
    "LZ%=:\n\t"
#define ATTN_ASM_CLOBBERS "v64", "v65", "v66", "v67", "v68", "v69", "v70", "v71", "v72", "v73", "v74", "v75", "v76", "v77", "v78", "v79", "v80", "v81", "v82", "v83", "v84", "v85", "v86", "v87", "v88", "v89", "v90", "v91", "v92", "v93", "v94", "v95", "v96", "v97", "v98", "v99", "v100", "v101", "v102", "v103", "v104", "v105", "v106", "v107", "v108", "v109", "v110", "v111", "v112", "v113", "v114", "v115", "v116", "v117", "v118", "v119", "v120", "v121", "v122", "v123", "v124", "v125", "v126", "v127", "v128", "v129", "v130", "v131", "v132", "v133", "v134", "v135", "v136", "v137", "v138", "v139", "v140", "v141", "v142", "v143", "v144", "v145", "v146", "v147", "v148", "v149", "v150", "v151", "v152", "v153", "v154", "v155", "v156", "v157", "v158", "v159", "v160", "v161", "v162", "v163", "v164", "v165", "v166", "v167", "v168", "v169", "v170", "v171", "v172", "v173", "v174", "v175", "v176", "v177", "v178", "v179", "v180", "v181", "v182", "v183", "v184", "v185", "v186", "v187", "v188", "v189", "v190", "v191", "v192", "v193", "v194", "v195", "v196", "v197", "v198", "v199", "v200", "v201", "v202", "v203", "v204", "v205", "v206", "v207", "v208", "v209", "v210", "v211", "v212", "v213", "v214", "v215", "v216", "v217", "v218", "v219", "v220", "v221", "v222", "v223", "v224", "v225", "v226", "v227", "v228"
DI f32x32 qk_step(const bf16x8& k0, const bf16x8& k1, const bf16x4& kt, const bf16x8& qm, const bf16x4& qt, const f32x16& c) {
    const f32x16 a = MFMA32(k0, qm, c), b = MFMA32(k1, qm, c);
    f32x32 s = __builtin_shufflevector(a, b, 0, 1, 2, 3, 4, 5, 6, 7, 8, 9, 10, 11, 12, 13, 14, 15, 16, 17, 18, 19, 20, 21, 22, 23, 24, 25, 26, 27, 28, 29, 30, 31);
    return MFMA2B(kt, qt, s);
}
DI float max32(const f32x32& s) {
    float m = fmaxf(s[0], s[1]);
#pragma unroll
    for (int r = 2; r < 32; r += 2) m = fmaxf(fmaxf(m, s[r]), s[r + 1]);
    return max_x32(m);
}
DI bf16x8 pack8(const f32x32& p, int b) { const u32x4 w = {cvtpk(p[b], p[b + 1]), cvtpk(p[b + 2], p[b + 3]), cvtpk(p[b + 4], p[b + 5]), cvtpk(p[b + 6], p[b + 7])}; return __builtin_bit_cast(bf16x8, w); }
DI unsigned tok_off(int t) { return (t >> 4) * 640 + ((t >> 2) & 1) * 320 + ((t & 3) + 4 * ((t >> 3) & 1)) * 2; }
DI unsigned ld2x2(rsrc_t r, unsigned off) { return (unsigned)__builtin_amdgcn_raw_buffer_load_b16(r, off, 0, 16) | ((unsigned)__builtin_amdgcn_raw_buffer_load_b16(r, off + 16, 0, 16) << 16); }
DI f32x16 attn_slow(rsrc_t wr, unsigned kv, int lane, bf16x8 qm, bf16x4 qt) {
    f32x16 o = {}; float m = -INFINITY; const f32x16 zero = {};
    const int r32 = lane & 31, h = lane >> 5; const unsigned voff = 2560 + h * 320 + (r32 < 20 ? r32 : 0) * 16; const unsigned pad = r32 == 20 ? 0x3F803F80u : 0u;
#pragma unroll 1
    for (int t = 0; t < 32; ++t) {
        const unsigned b = kv + t * KV_STEP;
        u32x4 k0, k1; u32x2 kt;
#pragma unroll
        for (int j = 0; j < 4; ++j) { k0[j] = ld2x2(wr, b + tok_off(r32) + (8 * h + 2 * j) * 16); k1[j] = ld2x2(wr, b + tok_off(32 + r32) + (8 * h + 2 * j) * 16); }
#pragma unroll
        for (int j = 0; j < 2; ++j) kt[j] = ld2x2(wr, b + tok_off(lane) + (16 + 2 * j) * 16);
        f32x32 s = qk_step(__builtin_bit_cast(bf16x8, k0), __builtin_bit_cast(bf16x8, k1), __builtin_bit_cast(bf16x4, kt), qm, qt, zero);
        const float mn = fmaxf(m, max32(s)); const float f = __builtin_amdgcn_exp2f(m - mn); m = mn;
#pragma unroll
        for (int r = 0; r < 16; ++r) o[r] *= f;
#pragma unroll
        for (int r = 0; r < 32; ++r) s[r] = __builtin_amdgcn_exp2f(s[r] - m);
#pragma unroll
        for (int j = 0; j < 4; ++j) { u32x4 v = ld16(wr, b + voff + j * 640); if (r32 >= 20) v = (u32x4){pad, pad, pad, pad}; o = MFMA32(__builtin_bit_cast(bf16x8, v), pack8(s, 8 * j), o); }
    }
    return o;
}
DI void p2_attention(unsigned lds0, int wid, int lane, int head, int qb, int ct80, char* ws, rsrc_t wr, const RtJob& j0, const RtJob& j1, const u16* rw0, const u16* rw1, u64 tag) {
    const int r32 = lane & 31, h = lane >> 5;
    const char* kv = ws + KV_OFF + head * KV_HEAD;
    const unsigned lo = lane * 16;
    const char* qp = ws + QT_OFF + head * Q_HEAD + qb * 10240;
    glds<1, true>(kv + wid * 1024, lo, lds0 + wid * 1024);
    glds<1, true>(qp + wid * 1024, lo, lds0 + (150 + wid) * 1024);
    if (wid < 2) glds<1, true>(qp + (8 + wid) * 1024, lo, lds0 + (158 + wid) * 1024);
#pragma unroll
    for (int p = 1; p < 4; ++p) glds<1, true>(kv + (p * 8 + wid) * 1024, lo, lds0 + (p * 8 + wid) * 1024);
    wait_vm<3>(); bar();
    const int G = lane >> 4, q4 = (lane & 15) >> 2, p4 = lane & 3;
    const unsigned ka = lds0 + (G & 1) * 640 + (p4 & 1) * 320 + (8 * (G >> 1) + q4) * 16 + (p4 >> 1) * 8;
    const unsigned ta = lds0 + G * 640 + (p4 & 1) * 320 + (16 + q4) * 16 + (p4 >> 1) * 8, va = lds0 + 2560 + h * 320 + r32 * 16;
    const unsigned qa = ka + 150 * 1024 + wid * 1280;
    const bf16x8 qmf = cat(lds_tr(qa), lds_tr(qa + 64)); const bf16x4 qtf = lds_tr(qa + (16 - 8 * (G >> 1)) * 16);
    const u32x4 qm = __builtin_bit_cast(u32x4, qmf); const u32x2 qt = __builtin_bit_cast(u32x2, qtf);
    float m0;
    { const f32x16 zero = {}; m0 = max32(qk_step(cat(lds_tr(ka), lds_tr(ka + 64)), cat(lds_tr(ka + 1280), lds_tr(ka + 1344)), lds_tr(ta), qmf, qtf, zero)); }
    wait_vm<0>(); bar_lgkm();
    const int wj = blockIdx.x * 8 + wid;
    f32x16 o = {};
    { unsigned cnt; float o0, o1, o2, o3, o4, o5, o6, o7, o8, o9, o10, o11;
      asm volatile(ATTN_ASM_BODY
          : [cnt] "=&s"(cnt), [o0] "=v"(o0), [o1] "=v"(o1), [o2] "=v"(o2), [o3] "=v"(o3), [o4] "=v"(o4), [o5] "=v"(o5), [o6] "=v"(o6), [o7] "=v"(o7), [o8] "=v"(o8), [o9] "=v"(o9), [o10] "=v"(o10), [o11] "=v"(o11)
          : [q0] "v"(qm[0]), [q1] "v"(qm[1]), [q2] "v"(qm[2]), [q3] "v"(qm[3]), [q4] "v"(qt[0]), [q5] "v"(qt[1]), [ka] "v"(ka), [ta] "v"(ta), [va] "v"(va), [lo] "v"(lo + (24 + wid) * 1024),
            [negm] "v"(-m0), [vinit] "v"(r32 == 20 ? 0x3F803F80u : 0u), [kv] "s"(kv), [ldsd] "s"(lds0 + (24 + wid) * 1024), [vmask] "s"(0x000FFFFF000FFFFFull),
            [wo] "s"(ws + WOT_OFF + ct80 * 100 * 1024), [ldsw] "s"(lds0 + wid * 1024 - 8192), [low] "v"(lo + wid * 1024 - 8192),
            [rs0] "v"(j0.src), [rt0] "v"(j0.src2), [rs1] "v"(j1.src), [rt1] "v"(j1.src2), [rw0] "s"(rw0), [rw1] "s"(rw1),
            [rd0] "v"(j0.dst), [rd1] "v"(j0.dst2), [rd2] "v"(j1.dst), [rd3] "v"(j1.dst2), [wr] "s"(wr), [sel0] "s"(0x05040100u), [sel1] "s"(0x07060302u),
            [wid] "s"(wid), [tgl] "s"((unsigned)tag), [tgh] "s"((unsigned)(tag >> 32)), [rtf] "v"(FR_OFF + blockIdx.x * 8), [rtc] "v"(FR_OFF + wid * 256 + (lane & 15) * 16),
            [po0] "v"(OI_OFF + wj * MARK_SHARE + lo), [po1] "v"(OI_OFF + wj * MARK_SHARE + lo + 4096), [po2] "v"(lo + 5120 < MARK_SHARE ? OI_OFF + wj * MARK_SHARE + lo + 5120 : 0xF0000000u)
          : "memory", "scc", "vcc",
            ATTN_ASM_CLOBBERS);
      o[0] = o0; o[1] = o1; o[2] = o2; o[3] = o3; o[4] = o4; o[5] = o5; o[6] = o6; o[7] = o7; o[8] = o8; o[9] = o9; o[10] = o10; o[11] = o11; }
    if (blockIdx.x == 0 && wid == 1 && lane == 0) st_flag(ws, FLG_OFF, tag);
    bool bad = false;
#pragma unroll
    for (int r = 0; r < 12; ++r) if (r < 9 || h == 0) bad |= !(fabsf(o[r]) < 1e30f);
    if (REDO_(__any(bad))) o = attn_slow(wr, KV_OFF + head * KV_HEAD, lane, qmf, qtf);
    const float inv = 1.f / __uint_as_float(__builtin_amdgcn_permlane32_swap(__float_as_uint(o[8]), __float_as_uint(o[8]), false, false)[1]);
    const int tok = qb * 256 + wid * 32 + r32, p = head >> 1, hb = head & 1;
    const unsigned base = OI_OFF + (tok >> 4) * (20 * 1024) + (tok & 15) * 16;
    st16(wr, base + (5 * p + 2 * hb + h) * 256, nomark4((u32x4){cvtpk(o[0] * inv, o[1] * inv), cvtpk(o[2] * inv, o[3] * inv), cvtpk(o[4] * inv, o[5] * inv), cvtpk(o[6] * inv, o[7] * inv)}));
    if (h == 0) st8(wr, base + (5 * p + 4) * 256 + hb * 8, (u32x2){nomark(cvtpk(o[8] * inv, o[9] * inv)), nomark(cvtpk(o[10] * inv, o[11] * inv))});
}

constexpr int G_UNIT = 10 * 1024, G_SLOT = 4 * G_UNIT, G_CSUM = 3 * G_SLOT + 7 * 5120;
DI void p4_compute(unsigned lds0, int wid, int lane, int rt, int ct, rsrc_t wr) {
    const int wn = wid & 1, wt = wid >> 1, tk = lane & 15, g = lane >> 4;
    unsigned ta[3], wa[3];
#pragma unroll
    for (int q = 0; q < 3; ++q) { ta[q] = lds0 + lane * 16 + (wt + q * 6) * G_UNIT; wa[q] = lds0 + lane * 16 + 4096 + (wn + q * 6) * G_UNIT; asm volatile("" : "+v"(ta[q]), "+v"(wa[q])); }
    f32x4 acc[6][4] = {};
    bf16x8 tf[2][4], wf[2][6];
    auto rdt = [&](int buf, int k, int i) { const int r = (2 * k + 12) & 15; tf[buf][i] = lds_frag(ta[r / 6] + (r % 6) * G_UNIT + i * 1024); };
    auto rdw = [&](int buf, int k, int j) { const int r = (2 * k + 12) & 15; wf[buf][j] = lds_frag(wa[r / 6] + (r % 6) * G_UNIT + j * 1024); };
    auto rd = [&](int buf, int k, int q) { if (q == 0) rdw(buf, k, 0); else if (q < 5) rdt(buf, k, q - 1); else rdw(buf, k, q - 4); };
    const unsigned ho = HI_OFF + rt * 163840 + wt * 4096 + lane * 16;
    bar();
#pragma unroll
    for (int q = 0; q < 10; ++q) rd(0, 0, q);
    float mu[4], sd[4];
    static_for<0, 20>([&](auto kk) {
        constexpr int k = kk.v, c = k & 1, n = c ^ 1, kn = k + 1;
        constexpr bool more = kn < 20, edge = (kn & 1) == 0 && more;
        SB();
        static_for<0, 24>([&](auto mm) {
            constexpr int m = mm.v, j = m >> 2, i = m & 3;
            acc[j][i] = MFMA16(wf[c][j], tf[c][i], acc[j][i]);
            if constexpr (edge) { if constexpr (m == 5) bar_lgkm(); if constexpr (m >= 5 && m < 15) rd(n, kn, m - 5); }
            else if constexpr (more && (m & 1) == 0 && m < 20) rd(n, kn, m >> 1);
            SB();
        });
    });
    if (REDO_(__any(acc[0][0][0] != acc[0][0][0] || acc[0][1][0] != acc[0][1][0] || acc[0][2][0] != acc[0][2][0] || acc[0][3][0] != acc[0][3][0]))) {
#pragma unroll
        for (int j = 0; j < 6; ++j)
#pragma unroll
            for (int i = 0; i < 4; ++i) acc[j][i] = (f32x4){};
        const unsigned wo_ = WGU_OFF + ct * 245760 + wn * 6144 + lane * 16;
#pragma unroll 1
        for (int k = 0; k < 20; ++k) {
            u32x4 a[4];
#pragma unroll
            for (int i = 0; i < 4; ++i) { a[i] = ld16(wr, ho + i * 1024, k * 8192); settle(wr, a[i], ho + i * 1024, k * 8192); }
#pragma unroll
            for (int j = 0; j < 6; ++j) { const bf16x8 w = __builtin_bit_cast(bf16x8, ld16(wr, wo_ + j * 1024, k * 12288));
#pragma unroll
                for (int i = 0; i < 4; ++i) acc[j][i] = MFMA16(w, __builtin_bit_cast(bf16x8, a[i]), acc[j][i]); }
        }
    }
    bar();
    float cs[6];
#pragma unroll
    for (int j = 0; j < 6; ++j) { const LAS float* cp = (const LAS float*)(uintptr_t)(lds0 + G_CSUM) + (6 * wn + j) * 16 + tk; cs[j] = (cp[0] + cp[192]) + (cp[384] + cp[576]); }
#pragma unroll
    for (int i = 0; i < 4; ++i) { const f32x2 t = *(const LAS f32x2*)(uintptr_t)(lds0 + G_CSUM + 3072 + ((4 * wt + i) * 16 + tk) * 8); mu[i] = t[0]; sd[i] = t[1]; }
    asm volatile("s_waitcnt lgkmcnt(0)\n\ts_barrier" : "+v"(cs[0]), "+v"(cs[1]), "+v"(cs[2]), "+v"(cs[3]), "+v"(cs[4]), "+v"(cs[5]), "+v"(mu[0]), "+v"(mu[1]), "+v"(mu[2]), "+v"(mu[3]),
                 "+v"(sd[0]), "+v"(sd[1]), "+v"(sd[2]), "+v"(sd[3]) :: "memory");
    { bf16x8 mb[4];
#pragma unroll
      for (int i = 0; i < 4; ++i) mb[i] = split_b(-mu[i], g);
#pragma unroll
      for (int j = 0; j < 6; ++j) { const bf16x8 ca = split_a(cs[j], g);
#pragma unroll
          for (int i = 0; i < 4; ++i) acc[j][i] = MFMA16(ca, mb[i], acc[j][i]); } }
    constexpr float C0 = -2.f * 1.4426950408889634f * 0.7978845608028654f, C1 = C0 * 0.044715f;
    float k0[4], k1[4], s2[4];
#pragma unroll
    for (int i = 0; i < 4; ++i) { const float rs = __builtin_amdgcn_rcpf(sd[i]); k0[i] = C0 * rs; k1[i] = C1 * rs * rs * rs; s2[i] = sd[i] * sd[i]; }
    const unsigned go = GI_OFF + (rt * 8 + 4 * wt + (g & 1)) * 49152 + (g >> 1) * 256 + tk * 16;
#pragma unroll
    for (int ip = 0; ip < 2; ++ip)
#pragma unroll
        for (int p = 0; p < 3; ++p) {
            unsigned y[2][2];
#pragma unroll
            for (int e = 0; e < 4; ++e) { const int i = 2 * ip + (e >> 1), r = 2 * (e & 1);
                const f32x2 a = {acc[2 * p][i][r], acc[2 * p][i][r + 1]}, t = a * (a * a * k1[i] + k0[i]);
                const f32x2 ex = {__builtin_amdgcn_exp2f(t[0]), __builtin_amdgcn_exp2f(t[1])}, d = ex * s2[i] + s2[i];
                const f32x2 z = a * (f32x2){acc[2 * p + 1][i][r], acc[2 * p + 1][i][r + 1]} * (f32x2){__builtin_amdgcn_rcpf(d[0]), __builtin_amdgcn_rcpf(d[1])};
                y[e >> 1][e & 1] = nomark(cvtpk(z[0], z[1])); }
            const auto s0 = __builtin_amdgcn_permlane16_swap(y[0][0], y[1][0], false, false), s1 = __builtin_amdgcn_permlane16_swap(y[0][1], y[1][1], false, false);
            const int f16 = ct * 6 + 3 * wn + p;
            st16(wr, go + ip * 98304 + (f16 >> 1) * 1024 + (f16 & 1) * 512, (u32x4){s0[0], s1[0], s0[1], s1[1]});
        }
}
DI void p4_issue_w(unsigned lds0, unsigned lo, const char* wb, int u) { glds<6, true>(wb + u * 6144, lo, lds0 + ((u + 12) & 15) * G_UNIT + 4096); }
DI void p4_issue_a(unsigned lds0, unsigned lo, const char* ab, int u) { glds<4, true>(ab + u * 4096, lo, lds0 + ((u + 12) & 15) * G_UNIT); }
DI void p5_issue_w(unsigned lds0, unsigned lo, const char* wdb, int st, int e) { glds<5, true>(wdb + ((e >> 2) * 24 + 4 * st + (e & 3)) * 5120, lo, lds0 + ((1 + st) & 3) * G_SLOT + e * 5120); }
DI void p4_loader(unsigned lds0, int lw, int lane, int rt, int ct, int ct80, char* ws, rsrc_t wr, unsigned tag32) {
    const unsigned lo = lane * 16;
    const char* ab = ws + HI_OFF + rt * 160 * 1024; const char* wb = ws + WGU_OFF + ct * 240 * 1024; const char* wdb = ws + WDT_OFF + ct80 * 240 * 1024;
    if (lw == 3) {
#pragma unroll
        for (int u = 0; u < 4; ++u) p4_issue_a(lds0, lo, ab, u);
    }
    p4_issue_a(lds0, lo, ab, 4 + lw);
    if (lw == 3) wait_vm<4>();
    bar();
    p4_issue_a(lds0, lo, ab, 8 + lw); p4_issue_a(lds0, lo, ab, 12 + lw);
    wait_vm<8>(); bar();
    p4_issue_w(lds0, lo, wb, 16 + lw); p4_issue_a(lds0, lo, ab, 16 + lw);
    wait_vm<14>(); bar();
#pragma unroll 1
    for (int s = 2; s < 6; ++s) {
        p4_issue_w(lds0, lo, wb, 4 * (s + 3) + lw); p4_issue_a(lds0, lo, ab, 4 * (s + 3) + lw);
        wait_vm<20>(); bar();
    }
    u32x4 pv[8]; u32x2 sp[4];
    const unsigned o = CS2_OFF + (ct * 16 + 4 * lw) * 1536 + lo, o2 = o + 1024 - (lane < 32 ? 0 : 512), so = ST2_OFF + ((lane >> 5) * 4 * 2048 + rt * 128 + lw * 32 + (lane & 31)) * 8;
#define PL_(d, a, off) "buffer_load_dwordx4 " d ", " a ", %14, 0 offen offset:" #off " sc1\n\t"
#define SL_(d, sof) "buffer_load_dwordx2 " d ", %17, %14, " sof " offen sc1\n\t"
    asm volatile(PL_("%0", "%12", 0) PL_("%1", "%12", 1536) PL_("%2", "%12", 3072) PL_("%4", "%13", 0) PL_("%5", "%13", 1536) PL_("%6", "%13", 3072) PL_("%3", "%15", 0) PL_("%7", "%16", 0)
                 SL_("%8", "0") SL_("%9", "%18") SL_("%10", "%19") SL_("%11", "%20")
                 : "=&v"(pv[0]), "=&v"(pv[1]), "=&v"(pv[2]), "=&v"(pv[3]), "=&v"(pv[4]), "=&v"(pv[5]), "=&v"(pv[6]), "=&v"(pv[7]), "=&v"(sp[0]), "=&v"(sp[1]), "=&v"(sp[2]), "=&v"(sp[3])
                 : "v"(o), "v"(o2), "s"(wr), "v"(o + 4608), "v"(o2 + 4608), "v"(so), "s"(16384), "s"(32768), "s"(49152) : "memory");
    p4_issue_w(lds0, lo, wb, 36 + lw); p4_issue_a(lds0, lo, ab, 36 + lw);
    wait_vm<32>(); bar();
    p5_issue_w(lds0, lo, wdb, 0, 2 * lw); p5_issue_w(lds0, lo, wdb, 0, 2 * lw + 1);
    asm volatile("s_waitcnt vmcnt(20)\n\ts_barrier" : "+v"(pv[0]), "+v"(pv[1]), "+v"(pv[2]), "+v"(pv[3]), "+v"(pv[4]), "+v"(pv[5]), "+v"(pv[6]), "+v"(pv[7]), "+v"(sp[0]), "+v"(sp[1]), "+v"(sp[2]), "+v"(sp[3]) :: "memory");
#pragma unroll 1
    for (int spin = 0; spin < SPIN_MAX; ++spin) {
        unsigned df = 0, mx = 0;
#pragma unroll
        for (int q = 0; q < 8; ++q) df |= (pv[q][1] ^ tag32) | (pv[q][3] ^ tag32);
#pragma unroll
        for (int q = 0; q < 4; ++q) mx = umax3(mx, sp[q][0], sp[q][1]);
        if (__all(df == 0 && mx != 0xFFFFFFFFu)) break;
        asm volatile("" ::: "memory");
#pragma unroll
        for (int q = 0; q < 4; ++q) { pv[q] = ld16(wr, o + q * 1536); pv[4 + q] = ld16(wr, o2 + q * 1536); sp[q] = __builtin_amdgcn_raw_buffer_load_b64(wr, so, q * 16384, 16); }
    }
    float m = 0.f, qq = 0.f;
#pragma unroll
    for (int q = 0; q < 4; ++q) m += __uint_as_float(sp[q][0]);
    m = add_x32(m) * 0.125f;
#pragma unroll
    for (int q = 0; q < 4; ++q) { const float d = __uint_as_float(sp[q][0]) - m; qq += __uint_as_float(sp[q][1]) + 80.f * d * d; }
    float mu, sd; ln_finish(m, add_x32(qq) * (1.f / DM), mu, sd);
    f32x2 z = {}, z2 = {};
#pragma unroll
    for (int q = 0; q < 4; ++q) { z += (f32x2){__uint_as_float(pv[q][0]), __uint_as_float(pv[q][2])}; z2 += (f32x2){__uint_as_float(pv[4 + q][0]), __uint_as_float(pv[4 + q][2])}; }
    asm volatile("s_waitcnt vmcnt(10)\n\ts_barrier" : "+v"(z), "+v"(z2), "+v"(mu), "+v"(sd) :: "memory");
    *(LAS f32x2*)(uintptr_t)(lds0 + G_CSUM + lw * 768 + lane * 8) = z;
    if (lane < 32) { *(LAS f32x2*)(uintptr_t)(lds0 + G_CSUM + lw * 768 + 512 + lane * 8) = z2; *(LAS f32x2*)(uintptr_t)(lds0 + G_CSUM + 3072 + (lw * 32 + lane) * 8) = (f32x2){mu, sd}; }
    p5_issue_w(lds0, lo, wdb, 1, 2 * lw); p5_issue_w(lds0, lo, wdb, 1, 2 * lw + 1);
    bar_lgkm();
    bar();
    p5_issue_w(lds0, lo, wdb, 2, 2 * lw); p5_issue_w(lds0, lo, wdb, 2, 2 * lw + 1);
    p5_issue_w(lds0, lo, wdb, 3, 2 * lw); p5_issue_w(lds0, lo, wdb, 3, 2 * lw + 1);
}

#define PM_DEPTH 1
#define A_AUX 16
#define OUT_AUX 0
#define OUT_ST(...) __builtin_amdgcn_raw_buffer_store_b64(__VA_ARGS__, __builtin_amdgcn_make_buffer_rsrc(out, 0, SEQ * DM * 2, 0x00020000), (tok * DM + ct80 * 80 + 4 * g) * 2 + 32 * j, 0, OUT_AUX)
#define P3_WIN 5
template <int K0, int NK, int NA, class WA, class PF> DI void proj_mma(f32x4 (&acc)[5], u32x4 (&a)[NA], WA&& wa, PF&& pf) {
    constexpr int D = PM_DEPTH;
    bf16x8 wf[D + 1][5];
#pragma unroll
    for (int d = 0; d < D; ++d)
#pragma unroll
        for (int j = 0; j < 5; ++j) wf[d][j] = lds_frag(wa(K0 + d) + j * 1024);
    static_for<K0, K0 + NK>([&](auto kk) {
        constexpr int k = kk.v, c = (k - K0) % (D + 1), n = (k - K0 + D) % (D + 1);
        SB();
#pragma unroll
        for (int j = 0; j < 5; ++j) { acc[j] = MFMA16(wf[c][j], __builtin_bit_cast(bf16x8, a[k]), acc[j]); if (k + D < K0 + NK) wf[n][j] = lds_frag(wa(k + D) + j * 1024); SB(); }
        pf(kk);
    });
}

template <int K> DI void ld16_raw(u32x4& v, rsrc_t wr, unsigned off) {
    asm volatile("buffer_load_dwordx4 %0, %1, %2, %3 offen offset:%4 sc1" : "=v"(v) : "v"(off), "s"(wr), "s"((K >> 2) * 4096), "n"((K & 3) * 1024) : "memory"); }
template <int N> DI void wait_for(u32x4& v) { asm volatile("s_waitcnt vmcnt(%1)" : "+v"(v) : "n"(N) : "memory"); }
DI void proj_redo(f32x4 (&acc)[5], rsrc_t wr, unsigned ao, unsigned wo_, int nk) {
#pragma unroll
    for (int j = 0; j < 5; ++j) acc[j] = (f32x4){};
#pragma unroll 1
    for (int k = 0; k < nk; ++k) {
        u32x4 a = ld16(wr, ao, k * 1024); settle(wr, a, ao, k * 1024);
#pragma unroll
        for (int j = 0; j < 5; ++j) acc[j] = MFMA16(__builtin_bit_cast(bf16x8, ld16(wr, wo_ + (j & 3) * 1024, k * 5120 + (j >> 2) * 4096)), __builtin_bit_cast(bf16x8, a), acc[j]);
    }
}

__global__ __launch_bounds__(512, 2) void k_block(const u16* x, const u16* wqkv, const u16* wo, const u16* wg, const u16* wu, const u16* wd, char* ws, u16* out) {
    __shared__ __attribute__((aligned(16))) char smem[160 * 1024];
    const int bid = blockIdx.x, xcd = bid & 7, idx = bid >> 3;
    const int lane = threadIdx.x & 63, wid = __builtin_amdgcn_readfirstlane(threadIdx.x >> 6);
    const unsigned lds0 = (unsigned)(uintptr_t)smem, lo = lane * 16;
    const rsrc_t wr = __builtin_amdgcn_make_buffer_rsrc(ws, 0, WS_END, 0x00020000);
    const int rt = (xcd >> 1) * 4 + (idx & 3), ct = (xcd & 1) * 8 + (idx >> 2);
    const int rt64 = xcd * 4 + (idx & 3), ct80 = idx >> 2;
    u32x2 gen, sv[5]; u32x4 cv[3];
    const int row0 = rt * 128 + ct * 8 + wid * 2, c0 = (ct < 15 ? ct : 0) * 128 + wid * 32;
    if (wid < 4) {
        const unsigned xo = ((row0 + (lane >> 5)) * DM + (lane & 31) * 4) * 2, wo_ = ((40 * rt + (lane >> 2)) * NQKV + c0 + (lane & 3) * 8) * 2;
        asm volatile("buffer_load_dwordx2 %0, %9, %10, 0 offen sc1\n\t"
                     "global_load_dwordx2 %1, %11, %12\n\tglobal_load_dwordx2 %2, %11, %12 offset:256\n\tglobal_load_dwordx2 %3, %11, %12 offset:512\n\tglobal_load_dwordx2 %4, %11, %12 offset:768\n\tglobal_load_dwordx2 %5, %11, %12 offset:1024\n\t"
                     "global_load_dwordx4 %6, %13, %16\n\tglobal_load_dwordx4 %7, %14, %16\n\tglobal_load_dwordx4 %8, %15, %16"
                     : "=&v"(gen), "=&v"(sv[0]), "=&v"(sv[1]), "=&v"(sv[2]), "=&v"(sv[3]), "=&v"(sv[4]), "=&v"(cv[0]), "=&v"(cv[1]), "=&v"(cv[2])
                     : "v"(FLG_OFF), "s"(wr), "v"(xo), "s"(x), "v"(wo_), "v"(wo_ + 16 * NQKV * 2), "v"(wo_ + (lane < 32 ? 32 : 0) * NQKV * 2), "s"(wqkv) : "memory");
        {
            const char* pc; int l1, l2;
            asm volatile("s_getpc_b64 %0\nblk_code_pc:\n\ts_mov_b32 %1, blk_code_end-blk_code_pc\n\ts_mov_b32 %2, blk_code_end2-blk_code_pc" : "=s"(pc), "=s"(l1), "=s"(l2));
            const int o = ((lane * 4 + wid) * 32 + idx) * 128;
            if (lane < 4 && o < (l1 > l2 ? l1 : l2)) touch(pc, o, lds0 + 159 * 1024);
        }
    }
    if (__builtin_expect(wid >= 4, 1)) {
        if (__builtin_expect(ct < 15, 1)) p1_loader(lds0, wid - 4, lane, rt, ct, x, wqkv);
        gen = ld8(wr, FLG_OFF);
    } else {
        asm volatile("s_waitcnt vmcnt(0)" : "+v"(gen), "+v"(sv[0]), "+v"(sv[1]), "+v"(sv[2]), "+v"(sv[3]), "+v"(sv[4]), "+v"(cv[0]), "+v"(cv[1]), "+v"(cv[2]) :: "memory");
        if (lane >= 32) cv[2] = (u32x4){0u, 0u, 0u, 0u};
        row_stats(sv, row0, lane, wr, gen[0] + 1);
        if (ct < 15) {
            float z = 0.f;
#pragma unroll
            for (int q = 0; q < 8; ++q) { const unsigned a = cv[0][q >> 1], b = cv[1][q >> 1], c = cv[2][q >> 1];
                const float t = add_x32(add_x16(dpp_add<0x128>(dpp_add<0x124>(q & 1 ? (bf_hi(a) + bf_hi(b)) + bf_hi(c) : (bf_lo(a) + bf_lo(b)) + bf_lo(c))))); z = (lane >> 2) == q ? t : z; }
            if (lane < 32) st8(wr, CS1_OFF + (rt * NQKV + c0 + (lane & 3) * 8 + (lane >> 2)) * 8, (u32x2){__float_as_uint(z), gen[0] + 1});
            p1_compute(lds0, wid, lane, rt * 128, ct, wr, gen[0] + 1);
        } else if (wid < 2) {
            const int c = rt * 120 + wid * 60 + (lane < 60 ? lane : 0);
            u32x2 pv[16];
#pragma unroll 1
            for (int spin = 0; spin < SPIN_MAX; ++spin) {
                unsigned df = 0;
#pragma unroll
                for (int q = 0; q < 16; ++q) pv[q] = __builtin_amdgcn_raw_buffer_load_b64(wr, CS1_OFF + c * 8, q * NQKV * 8, 16);
#pragma unroll
                for (int q = 0; q < 16; ++q) df |= pv[q][1] ^ (gen[0] + 1);
                if (__all(df == 0)) break;
                asm volatile("s_sleep 8" ::: "memory");
            }
            float z = 0.f;
#pragma unroll
            for (int q = 0; q < 16; ++q) z += __uint_as_float(pv[q][0]);
            st8(wr, CF1_OFF + c * 8, (u32x2){__float_as_uint(z), gen[0] + 1});
        }
        asm volatile("blk_code_end2:");
    }
    const u64 tag = uni64(gen) + 1;
    int s0, s1; RtJob j0 = retile_decode<true>(bid * 8 + wid, lane, s0), j1 = retile_decode<false>(bid * 8 + wid, lane, s1);
    asm volatile("" : "+v"(j0.src), "+v"(j0.src2), "+v"(j0.dst), "+v"(j0.dst2), "+v"(j1.src), "+v"(j1.src2), "+v"(j1.dst), "+v"(j1.dst2));
    bar_drain();
    const int head = xcd * 4 + (idx >> 3), qb = idx & 7;
    if (wid == 1 && lane == 0) st_flag(ws, F1_OFF + (ct * 16 + rt) * F1_STRIDE, tag);
    if (wid == 0) {
        const int cl = (20 * head + ((lane >> 4) & 1) * 19) >> 7;
        const unsigned o1 = F1_OFF + ((5 + 5 * (lane >> 5) + cl) * 16 + (lane & 15)) * F1_STRIDE, o2 = F1_OFF + (cl * 16 + 2 * qb + (lane & 1)) * F1_STRIDE;
#pragma unroll 1
        for (int spin = 0; spin < SPIN_MAX; ++spin) {
            const u32x2 a = ld8(wr, o1), b = ld8(wr, o2);
            if (__all(((a[0] ^ (unsigned)tag) | (a[1] ^ (unsigned)(tag >> 32)) | (b[0] ^ (unsigned)tag) | (b[1] ^ (unsigned)(tag >> 32))) == 0)) break;
            asm volatile("s_sleep %0" :: "n"(POLL_SLEEP) : "memory");
        }
        asm volatile("" ::: "memory");
    }
    bar();
    p2_attention(lds0, wid, lane, head, qb, ct80, ws, wr, j0, j1, s0 ? wu : wg, s1 ? wo : wd, tag);
    bar();
    const int tt = wid & 3, kh = wid >> 2, tk = lane & 15, g = lane >> 4;
    const int tok = rt64 * 64 + tt * 16 + tk;
    u32x2 res[5];
    constexpr unsigned GU_CP = 15 * G_UNIT;
    if (kh == 1) {
        const int ch = lane & 31, u = ch >= 12, c = ch < 24 ? ch - 12 * u : 0;
        const u16* p = (u ? wu : wg) + (40 * rt + 10 * tt + (lane >> 5)) * FF + 96 * ct + c * 8;
        u32x4 cv[5];
#pragma unroll
        for (int i = 0; i < 5; ++i) cv[i] = *(const u32x4*)(p + 2 * i * FF);
        f32x2 s[4] = {};
#pragma unroll
        for (int i = 0; i < 5; ++i)
#pragma unroll
            for (int q = 0; q < 4; ++q) s[q] += (f32x2){bf_lo(cv[i][q]), bf_hi(cv[i][q])};
        f32x4 z;
#pragma unroll
        for (int q = 0; q < 4; ++q) { const float a = add_x32(s[q >> 1][q & 1]), b = add_x32(s[2 + (q >> 1)][q & 1]); z[q] = lane < 32 ? a : b; }
        if (ch < 24) *(LAS f32x4*)((LAS float*)(uintptr_t)(lds0 + GU_CP) + tt * 192 + (c >> 1) * 32 + 16 * u + (c & 1) * 8 + (lane >> 5) * 4) = z;
        p4_issue_w(lds0, lo, ws + WGU_OFF + ct * 240 * 1024, tt);
    } else {
#pragma unroll
        for (int j = 0; j < 5; ++j) res[j] = *(const u32x2*)(x + tok * DM + ct80 * 80 + 16 * j + 4 * g);
    }
    if (wid == 0) poll_mark(wr, OI_OFF + (rt64 * 4 + (lane >> 5) * 2) * 20480 + (5 * ((lane & 31) >> 1) + 4) * 256 + (lane & 1) * 8 + 244);
    bar_drain();
    const unsigned scr = lds0 + lo;
    auto scr_of = [&](int j) { const int q = tt * 5 + j; return (10 + (q >> 2)) * G_UNIT + (q & 3) * 1024; };
    {
        u32x4 a[10];
        const unsigned ao = OI_OFF + ((rt64 * 4 + tt) * 20 + 10 * kh) * 1024 + lo;
        auto lda = [&](int k) { a[k] = __builtin_amdgcn_raw_buffer_load_b128(wr, ao + (k & 3) * 1024, (k >> 2) * 4096, A_AUX); };
#pragma unroll
        for (int k = 0; k < P3_WIN; ++k) lda(k);
        if (wid == 5 || wid == 6) { const int t2 = (wid - 5) * 64 + lane; f32x2 z = {};
#pragma unroll
            for (int q = 0; q < 4; ++q) z += *(const LAS f32x2*)(uintptr_t)(lds0 + GU_CP + q * 768 + (t2 < 96 ? t2 : 0) * 8);
            if (t2 < 96) st16(wr, CS2_OFF + (ct * 16 + rt) * 1536 + t2 * 16, (u32x4){__float_as_uint(z[0]), (unsigned)tag, __float_as_uint(z[1]), (unsigned)tag}); }
        f32x4 acc[5] = {};
        const unsigned wa = lds0 + lo + kh * 10 * 5120;
        proj_mma<0, 10>(acc, a, [&](int k) { return wa + k * 5120; }, [&](auto kk) { if constexpr (kk.v + P3_WIN < 10) lda(kk.v + P3_WIN); });
        if (REDO_(__any(acc[0][0] != acc[0][0]))) proj_redo(acc, wr, ao, WOT_OFF + (ct80 * 20 + 10 * kh) * 5120 + lo, 10);
        if (kh == 1) {
#pragma unroll
            for (int j = 0; j < 5; ++j) *(LAS f32x4*)(uintptr_t)(scr + scr_of(j)) = acc[j];
        }
        bar_lgkm();
        if (kh == 1) {
            const char* wb = ws + WGU_OFF + ct * 240 * 1024;
            if (tt < 3) {
#pragma unroll
                for (int s = 0; s < 4; ++s) p4_issue_w(lds0, lo, wb, 4 + 3 * s + tt);
            } else {
#define POLL_DELAY 40
                asm volatile("s_sleep %0" :: "n"(POLL_DELAY) : "memory");
                poll_mark(wr, HI_OFF + rt * 163840 + (((lane & 7) * 5 + 4) >> 1) * 8192 + (lane & 1) * 512 + (lane >> 3) * 1024 + 508); }
        } else {
            float v[20]; float s = 0.f;
            const unsigned ho = HI_OFF + (rt64 >> 1) * 163840 + ((rt64 & 1) * 4 + tt) * 1024 + (g >> 1) * 256 + tk * 16 + (g & 1) * 8;
#pragma unroll
            for (int j = 0; j < 5; ++j) { const f32x4 p = *(const LAS f32x4*)(uintptr_t)(scr + scr_of(j));
                res[j] = (u32x2){cvtpk(acc[j][0] + p[0] + bf_lo(res[j][0]), acc[j][1] + p[1] + bf_hi(res[j][0])), cvtpk(acc[j][2] + p[2] + bf_lo(res[j][1]), acc[j][3] + p[3] + bf_hi(res[j][1]))};
                res[j] = (u32x2){nomark(res[j][0]), nomark(res[j][1])};
                __builtin_amdgcn_raw_buffer_store_b64(res[j], wr, ho, ((ct80 * 5 + j) >> 1) * 8192 + ((ct80 * 5 + j) & 1) * 512, 16);
                v[4 * j] = bf_lo(res[j][0]); v[4 * j + 1] = bf_hi(res[j][0]); v[4 * j + 2] = bf_lo(res[j][1]); v[4 * j + 3] = bf_hi(res[j][1]);
                s += (v[4 * j] + v[4 * j + 1]) + (v[4 * j + 2] + v[4 * j + 3]); }
            const float m = add_x32(add_x16(s)) * (1.f / 80.f); float q = 0.f;
#pragma unroll
            for (int j = 0; j < 20; ++j) { const float d = v[j] - m; q += d * d; }
            q = add_x32(add_x16(q));
            if (g == 0) st8(wr, ST2_OFF + (ct80 * 2048 + tok) * 8, (u32x2){nomark(__float_as_uint(m)), nomark(__float_as_uint(q))});
        }
    }
    bar_lgkm();
    if (kh == 1) p4_loader(lds0, tt, lane, rt, ct, ct80, ws, wr, (unsigned)tag);
    else { p4_compute(lds0, wid, lane, rt, ct, wr); }
    if (wid == 0) poll_mark(wr, GI_OFF + ((rt64 * 4 + 2 + (lane >> 5)) * 48 + (lane & 15) * 3 + 1 + ((lane >> 4) & 1)) * 1024 + ((lane >> 4) & 1) * 512 + 508);
    if (kh == 1) asm volatile("s_waitcnt vmcnt(0)" ::: "memory");
    bar();
    {
        u32x4 a[24];
        const unsigned ao = GI_OFF + ((rt64 * 4 + tt) * 48 + 24 * kh) * 1024 + lo;
        auto lda = [&](auto kk) { ld16_raw<kk.v>(a[kk.v], wr, ao); };
        static_for<0, 8>(lda);
        f32x4 acc[5] = {};
        unsigned wa[4];
#pragma unroll
        for (int q = 0; q < 4; ++q) { wa[q] = lds0 + lo + kh * 4 * 5120 + q * G_SLOT; asm volatile("" : "+v"(wa[q])); }
        const char* wdb = ws + WDT_OFF + ct80 * 240 * 1024;
        bf16x8 wf[2][5];
        auto rdw = [&](int k, int j) { wf[k & 1][j] = lds_frag(wa[(1 + (k >> 2)) & 3] + (k & 3) * 5120 + j * 1024); };
#pragma unroll
        for (int j = 0; j < 5; ++j) rdw(0, j);
        static_for<0, 24>([&](auto kk) {
            constexpr int k = kk.v, N = k < 4 ? 7 : k < 8 ? 12 : k < 12 ? 17 : k < 16 ? 12 : 23 - k;
            wait_for<N>(a[k]);
            SB();
#pragma unroll
            for (int j = 0; j < 5; ++j) { acc[j] = MFMA16(wf[k & 1][j], __builtin_bit_cast(bf16x8, a[k]), acc[j]); if (k + 1 < 24 && k != 15) rdw(k + 1, j); SB(); }
            if constexpr (k + 8 < 24) lda(IC<k + 8>{});
            if constexpr (k == 3) { bar(); p5_issue_w(lds0, lo, wdb, 4, wid); }
            if constexpr (k == 7) { bar(); p5_issue_w(lds0, lo, wdb, 5, wid); }
            if constexpr (k == 15) { wait_vm<8>(); bar();
#pragma unroll
                for (int j = 0; j < 5; ++j) rdw(16, j); }
        });
        if (REDO_(__any(acc[0][0] != acc[0][0]))) proj_redo(acc, wr, ao, WDT_OFF + (ct80 * 48 + 24 * kh) * 5120 + lo, 24);
        if (kh == 1) {
#pragma unroll
            for (int j = 0; j < 5; ++j) *(LAS f32x4*)(uintptr_t)(scr + (tt * 5 + j) * 1024) = acc[j];
        }
        bar_lgkm();
        if (kh == 0) {
#pragma unroll
            for (int j = 0; j < 5; ++j) { const f32x4 p = *(const LAS f32x4*)(uintptr_t)(scr + (tt * 5 + j) * 1024);
                OUT_ST((u32x2){cvtpk(acc[j][0] + p[0] + bf_lo(res[j][0]), acc[j][1] + p[1] + bf_hi(res[j][0])), cvtpk(acc[j][2] + p[2] + bf_lo(res[j][1]), acc[j][3] + p[3] + bf_hi(res[j][1]))}); }
        }
    }
    asm volatile("blk_code_end:");
}

}

extern "C" void kernel_launch(void* const* d_in, const int* in_sizes, int n_in, void* d_out, int out_size, void* d_ws, size_t ws_size, hipStream_t stream) {
    using namespace blk;
    (void)in_sizes; (void)n_in; (void)out_size; (void)ws_size;
    k_block<<<256, 512, 0, stream>>>((const u16*)d_in[0], (const u16*)d_in[1], (const u16*)d_in[2], (const u16*)d_in[4], (const u16*)d_in[5], (const u16*)d_in[6], (char*)d_ws, (u16*)d_out);
}
```
